```python
import torch

def _softmax(x, axis=-1):
    return torch.softmax(x, dim=axis)

def _gelu(x):
    return torch.nn.functional.gelu(x, approximate='tanh')

def _silu(x):
    return torch.nn.functional.silu(x)

def _sigmoid(x):
    return torch.sigmoid(x)

def _layernorm(x, axis=-1, eps=1e-05):
    m = x.mean(dim=axis, keepdim=True)
    v = x.var(dim=axis, keepdim=True, unbiased=False)
    return (x - m) / torch.sqrt(v + eps)

def _scatter_add(tgt, idx, src, axis=0):
    return tgt.clone().index_add_(axis, idx, src)

def _segment_sum(src, seg, n):
    out = torch.zeros((n,) + src.shape[1:], dtype=src.dtype)
    return out.index_add_(0, seg, src)

def _pad(x, pw):
    flat = tuple((v for pair in reversed(pw) for v in pair))
    return torch.nn.functional.pad(x, flat)
kernel_name = 'standalone__factorization_afm__recsys'

def setup_inputs():

    _w = lambda a: (lambda t: t.to(torch.bfloat16) if t.is_floating_point() else t)(torch.as_tensor(a))
    B, F, D, V = (4096, 26, 128, min(1000000, 100000))
    A = D
    ii, jj = tuple(torch.triu_indices(F, F, 1).unbind(0))
    return {'idx': _w((lambda lo, hi, *s: torch.randint(lo, hi, s))(0, V, B, F).to(torch.int64)), 'table': _w((lambda *s: torch.randn(*s, dtype=torch.bfloat16) * 0.2)(V, D)), 'ii': _w(ii.to(torch.int64)), 'jj': _w(jj.to(torch.int64)), 'W': _w((lambda *s: torch.randn(*s, dtype=torch.bfloat16) * 1.6)(D, A) * (1.0 / D ** 0.5)), 'b': _w((lambda *s: torch.randn(*s, dtype=torch.bfloat16) * 1.6)(A)), 'h': _w((lambda *s: torch.randn(*s, dtype=torch.bfloat16) * 1.6)(A)), 'p': _w((lambda *s: torch.randn(*s, dtype=torch.bfloat16) * 0.2)(D))}

def reference(idx, table, ii, jj, W, b, h, p):
    emb = table[idx]
    ei = emb[:, ii, :].clone()
    ej = emb[:, jj, :].clone()
    pair = ei * ej
    hid = pair @ W + b
    hid = 0.5 * (hid + torch.abs(hid))
    e = hid @ h
    alpha = _softmax(e, axis=-1)
    pooled = torch.einsum('bp,bpd->bd', alpha, pair)
    return pooled @ p
```

```cpp
#include <hip/hip_runtime.h>
#include <hip/hip_bf16.h>
#include <stdint.h>

static constexpr int F = 26, D = 128, A = 128, P = 325;

__device__ __forceinline__ float bf2f(uint16_t x) { return __uint_as_float(((uint32_t)x) << 16); }

__global__ __launch_bounds__(256) void naive_kernel(const int* __restrict__ idx, const uint16_t* __restrict__ table,
    const int* __restrict__ ii, const int* __restrict__ jj, const uint16_t* __restrict__ W, const uint16_t* __restrict__ b,
    const uint16_t* __restrict__ h, const uint16_t* __restrict__ p, __hip_bfloat16* __restrict__ out, int B) {
  __shared__ float E[F][D + 1];
  __shared__ float Ws[D][A];
  __shared__ float ev[P], sv[P];
  __shared__ float red[256];
  int s = blockIdx.x;
  int t = threadIdx.x;
  for (int i = t; i < D * A; i += 256) Ws[i / A][i % A] = bf2f(W[i]);
  for (int i = t; i < F * D; i += 256) { int f = i / D, d = i % D; E[f][d] = bf2f(table[(size_t)idx[s * F + f] * D + d]); }
  __syncthreads();
  for (int q = t; q < P; q += 256) {
    int i = ii[q], j = jj[q];
    float e = 0.f, sacc = 0.f;
    for (int d = 0; d < D; ++d) sacc += E[i][d] * E[j][d] * bf2f(p[d]);
    for (int a = 0; a < A; ++a) {
      float x = 0.f;
      for (int d = 0; d < D; ++d) x += E[i][d] * E[j][d] * Ws[d][a];
      x += bf2f(b[a]);
      x = x > 0.f ? x : 0.f;
      e += x * bf2f(h[a]);
    }
    ev[q] = e; sv[q] = sacc;
  }
  __syncthreads();
  if (t == 0) {
    float m = -1e30f;
    for (int q = 0; q < P; ++q) m = fmaxf(m, ev[q]);
    float l = 0.f, n = 0.f;
    for (int q = 0; q < P; ++q) { float w = __expf(ev[q] - m); l += w; n += w * sv[q]; }
    out[s] = __float2bfloat16(n / l);
  }
}

extern "C" void kernel_launch(void* const* d_in, const int* in_sizes, int n_in,
                              void* d_out, int out_size, void* d_ws, size_t ws_size,
                              hipStream_t stream) {
  const int* idx = (const int*)d_in[0];
  const uint16_t* table = (const uint16_t*)d_in[1];
  const int* ii = (const int*)d_in[2];
  const int* jj = (const int*)d_in[3];
  const uint16_t* W = (const uint16_t*)d_in[4];
  const uint16_t* b = (const uint16_t*)d_in[5];
  const uint16_t* h = (const uint16_t*)d_in[6];
  const uint16_t* p = (const uint16_t*)d_in[7];
  int B = out_size;
  naive_kernel<<<B, 256, 0, stream>>>(idx, table, ii, jj, W, b, h, p, (__hip_bfloat16*)d_out, B);
}
```

```cpp
#include <hip/hip_runtime.h>
#include <hip/hip_bf16.h>
#include <stdint.h>

namespace {
constexpr int F = 26, D = 128, A = 128, NP = 325;
constexpr int WAVE0 = 38272, WS = 30016, RING = 0, CX = 38016, LDS_TOTAL = 158336;

__device__ const uint16_t g_lane_tab[64 * 16] = {2880,2592,6336,6624,26496,27136,27472,27792,28096,29152,13824,15264,15552,0,0,0,288,0,6048,6624,26576,27216,27552,27872,28100,29312,13824,15840,15840,0,0,0,288,576,5472,4896,26656,27296,27632,27952,28104,29236,14112,15552,15552,0,0,0,2880,3168,4320,3744,26736,27376,27712,28032,28108,29488,14976,16416,16416,0,0,0,1440,1152,3456,4032,26500,27140,27476,27796,28112,29152,15552,16992,17280,0,0,0,576,0,4608,4896,26580,27220,27556,27876,28116,29312,15552,17568,17568,0,0,0,1728,2016,1728,1440,26660,27300,27636,27956,28120,29236,15840,17280,17280,0,0,0,864,288,2016,1152,26740,27380,27716,28036,28124,29488,16704,18144,18144,0,0,0,2592,3168,5184,4896,26504,27144,27480,27800,28176,29152,17280,18720,19008,0,0,0,2304,2880,3456,4320,26584,27224,27560,27880,28180,29312,17280,19296,19296,0,0,0,864,0,4608,5184,26664,27304,27640,27960,28184,29236,17568,19008,19008,0,0,0,2304,3168,3744,4032,26744,27384,27720,28040,28188,29488,18432,19872,19872,0,0,0,864,576,5184,5472,26508,27148,27484,27804,28192,29152,19008,20448,20736,0,0,0,1440,2016,3456,3744,26588,27228,27564,27884,28196,29312,19008,21024,21024,0,0,0,2304,2592,4320,4032,26668,27308,27644,27964,28200,29236,19296,20736,20736,0,0,0,1728,1152,4608,5472,26748,27388,27724,28044,28204,29488,20160,21600,21600,0,0,0,2896,2608,6352,6640,26816,27152,27488,27808,28128,29232,13840,15280,15568,0,0,0,304,16,6064,6640,26896,27232,27568,27888,28132,29232,13840,15856,15856,0,0,0,304,592,5488,4912,26976,27312,27648,27968,28136,29232,14128,15568,15568,0,0,0,2896,3184,4336,3760,27056,27392,27728,28048,28140,29232,14992,16432,16432,0,0,0,1456,1168,3472,4048,26820,27156,27492,27812,28144,29232,15568,17008,17296,0,0,0,592,16,4624,4912,26900,27236,27572,27892,28148,29232,15568,17584,17584,0,0,0,1744,2032,1744,1456,26980,27316,27652,27972,28152,29232,15856,17296,17296,0,0,0,880,304,2032,1168,27060,27396,27732,28052,28156,29232,16720,18160,18160,0,0,0,2608,3184,5200,4912,26824,27160,27496,27816,28208,29232,17296,18736,19024,0,0,0,2320,2896,3472,4336,26904,27240,27576,27896,28212,29232,17296,19312,19312,0,0,0,880,16,4624,5200,26984,27320,27656,27976,28216,29232,17584,19024,19024,0,0,0,2320,3184,3760,4048,27064,27400,27736,28056,28220,29232,18448,19888,19888,0,0,0,880,592,5200,5488,26828,27164,27500,27820,28224,29232,19024,20464,20752,0,0,0,1456,2032,3472,3760,26908,27244,27580,27900,28228,29232,19024,21040,21040,0,0,0,2320,2608,4336,4048,26988,27324,27660,27980,28232,29232,19312,20752,20752,0,0,0,1744,1168,4624,5488,27068,27404,27740,28060,28236,29232,20176,21616,21616,0,0,0,2912,2624,6368,6656,26832,27168,27504,27824,29376,29232,13856,15296,15584,0,0,0,320,32,6080,6656,26912,27248,27584,27904,29380,29232,13856,15872,15872,0,0,0,320,608,5504,4928,26992,27328,27664,27984,29384,29232,14144,15584,15584,0,0,0,2912,3200,4352,3776,27072,27408,27744,28064,29388,29232,15008,16448,16448,0,0,0,1472,1184,3488,4064,26836,27172,27508,27828,29392,29232,15584,17024,17312,0,0,0,608,32,4640,4928,26916,27252,27588,27908,29396,29232,15584,17600,17600,0,0,0,1760,2048,1760,1472,26996,27332,27668,27988,29400,29232,15872,17312,17312,0,0,0,896,320,2048,1184,27076,27412,27748,28068,29404,29232,16736,18176,18176,0,0,0,2624,3200,5216,4928,26840,27176,27512,27832,29456,29232,17312,18752,19040,0,0,0,2336,2912,3488,4352,26920,27256,27592,27912,29460,29232,17312,19328,19328,0,0,0,896,32,4640,5216,27000,27336,27672,27992,29464,29232,17600,19040,19040,0,0,0,2336,3200,3776,4064,27080,27416,27752,28072,29468,29232,18464,19904,19904,0,0,0,896,608,5216,5504,26844,27180,27516,27836,29472,29232,19040,20480,20768,0,0,0,1472,2048,3488,3776,26924,27260,27596,27916,29476,29232,19040,21056,21056,0,0,0,2336,2624,4352,4064,27004,27340,27676,27996,29480,29232,19328,20768,20768,0,0,0,1760,1184,4640,5504,27084,27420,27756,28076,29484,29232,20192,21632,21632,0,0,0,2928,2640,6384,6672,27012,27456,27776,29056,29320,29232,13872,15312,15600,0,0,0,336,48,6096,6672,26256,27536,27856,29136,29316,29232,13872,15888,15888,0,0,0,336,624,5520,4944,26340,27616,27936,29216,28980,29232,14160,15600,15600,0,0,0,2928,3216,4368,3792,27096,27696,28016,29296,27428,29232,15024,16464,16464,0,0,0,1488,1200,3504,4080,26592,27460,27780,29060,27344,29232,15600,17040,17328,0,0,0,624,48,4656,4944,26336,27540,27860,29140,28816,29232,15600,17616,17616,0,0,0,1776,2064,1776,1488,26760,27620,27940,29220,26676,29232,15888,17328,17328,0,0,0,912,336,2064,1200,26420,27700,28020,29300,26752,29232,16752,18192,18192,0,0,0,2640,3216,5232,4944,27092,27464,27784,29064,28900,29232,17328,18768,19056,0,0,0,2352,2928,3504,4368,27008,27544,27864,29144,27424,29232,17328,19344,19344,0,0,0,912,48,4656,5232,26416,27624,27944,29224,28896,29232,17616,19056,19056,0,0,0,2352,3216,3792,4080,27088,27704,28024,29304,27348,29232,18480,19920,19920,0,0,0,912,624,5232,5520,26424,27468,27788,29068,28984,29232,19056,20496,20784,0,0,0,1488,2064,3504,3792,26756,27548,27868,29148,27264,29232,19056,21072,21072,0,0,0,2352,2640,4368,4080,26928,27628,27948,29228,27432,29232,19344,20784,20784,0,0,0,1776,1200,4656,5520,26672,27708,28028,29308,28976,29232,20208,21648,21648,0,0,0};
__device__ const uint32_t g_pair_tab[328] = {256,512,768,1024,1280,1536,1792,2048,2304,2560,2816,3072,3328,3584,3840,4096,4352,4608,4864,5120,5376,5632,5888,6144,6400,513,769,1025,1281,1537,1793,2049,2305,2561,2817,3073,3329,3585,3841,4097,4353,4609,4865,5121,5377,5633,5889,6145,6401,770,1026,1282,1538,1794,2050,2306,2562,2818,3074,3330,3586,3842,4098,4354,4610,4866,5122,5378,5634,5890,6146,6402,1027,1283,1539,1795,2051,2307,2563,2819,3075,3331,3587,3843,4099,4355,4611,4867,5123,5379,5635,5891,6147,6403,1284,1540,1796,2052,2308,2564,2820,3076,3332,3588,3844,4100,4356,4612,4868,5124,5380,5636,5892,6148,6404,1541,1797,2053,2309,2565,2821,3077,3333,3589,3845,4101,4357,4613,4869,5125,5381,5637,5893,6149,6405,1798,2054,2310,2566,2822,3078,3334,3590,3846,4102,4358,4614,4870,5126,5382,5638,5894,6150,6406,2055,2311,2567,2823,3079,3335,3591,3847,4103,4359,4615,4871,5127,5383,5639,5895,6151,6407,2312,2568,2824,3080,3336,3592,3848,4104,4360,4616,4872,5128,5384,5640,5896,6152,6408,2569,2825,3081,3337,3593,3849,4105,4361,4617,4873,5129,5385,5641,5897,6153,6409,2826,3082,3338,3594,3850,4106,4362,4618,4874,5130,5386,5642,5898,6154,6410,3083,3339,3595,3851,4107,4363,4619,4875,5131,5387,5643,5899,6155,6411,3340,3596,3852,4108,4364,4620,4876,5132,5388,5644,5900,6156,6412,3597,3853,4109,4365,4621,4877,5133,5389,5645,5901,6157,6413,3854,4110,4366,4622,4878,5134,5390,5646,5902,6158,6414,4111,4367,4623,4879,5135,5391,5647,5903,6159,6415,4368,4624,4880,5136,5392,5648,5904,6160,6416,4625,4881,5137,5393,5649,5905,6161,6417,4882,5138,5394,5650,5906,6162,6418,5139,5395,5651,5907,6163,6419,5396,5652,5908,6164,6420,5653,5909,6165,6421,5910,6166,6422,6167,6423,6424,6424,6424,6424};

__device__ __forceinline__ float bf2f(uint32_t x) { return __uint_as_float(x << 16); }
__device__ __forceinline__ float wmax(float v) {
#pragma unroll
  for (int o = 32; o > 0; o >>= 1) v = fmaxf(v, __shfl_xor(v, o, 64));
  return v;
}
__device__ __forceinline__ float wsum(float v) {
#pragma unroll
  for (int o = 32; o > 0; o >>= 1) v += __shfl_xor(v, o, 64);
  return v;
}
__device__ __forceinline__ float p2(int e) {
  e = e < -126 ? -126 : (e > 127 ? 127 : e);
  return __uint_as_float((uint32_t)(e + 127) << 23);
}

__device__ __forceinline__ void slow_sample(const int* __restrict__ idx, const uint16_t* __restrict__ table, const int* __restrict__ ii,
                                            const int* __restrict__ jj, const uint16_t* __restrict__ W, const uint16_t* __restrict__ b,
                                            const uint16_t* __restrict__ h, const uint16_t* __restrict__ p, float* E, int s, int lane,
                                            uint16_t* __restrict__ out) {
  for (int q = lane; q < F * D; q += 64) E[q] = bf2f(table[(size_t)idx[s * F + q / D] * D + q % D]);
  __builtin_amdgcn_s_waitcnt(0);
  __builtin_amdgcn_wave_barrier();
  float m = -INFINITY, l = 0.f, n = 0.f;
  for (int q = lane; q < NP; q += 64) {
    const float* Ei = E + ii[q] * D;
    const float* Ej = E + jj[q] * D;
    float e = 0.f, sv = 0.f;
    for (int d = 0; d < D; ++d) sv += Ei[d] * Ej[d] * bf2f(p[d]);
    for (int a = 0; a < A; ++a) {
      float x = 0.f;
      for (int d = 0; d < D; ++d) x += Ei[d] * Ej[d] * bf2f(W[d * A + a]);
      x += bf2f(b[a]);
      e += fmaxf(x, 0.f) * bf2f(h[a]);
    }
    float mn = fmaxf(m, e);
    float sc = __expf(m - mn), w = __expf(e - mn);
    l = l * sc + w; n = n * sc + w * sv; m = mn;
  }
  float M = wmax(m);
  float sc = (m == -INFINITY) ? 0.f : __expf(m - M);
  l = wsum(l * sc); n = wsum(n * sc);
  if (lane == 0) out[s] = __builtin_bit_cast(uint16_t, __float2bfloat16(n / l));
  __builtin_amdgcn_s_waitcnt(0);
  __builtin_amdgcn_wave_barrier();
}

__global__ __launch_bounds__(256, 1) void afm_kernel(const int* __restrict__ idx, const uint16_t* __restrict__ table,
    const int* __restrict__ ii, const int* __restrict__ jj, const uint16_t* __restrict__ W, const uint16_t* __restrict__ b,
    const uint16_t* __restrict__ h, const uint16_t* __restrict__ p, uint16_t* __restrict__ out, int B) {
  asm volatile(
      "s_getpc_b64 s[40:41]\n\t"
      "Lafm_pc:\n\t"
      "v_lshlrev_b32 v243, 7, v0\n\t"
      "v_min_u32 v243, Lafm_codeend-Lafm_pc-4, v243\n\t"
      "global_load_dword v243, v243, s[40:41]\n\t" ::: "s40", "s41", "v243");
  __builtin_amdgcn_sched_barrier(0);
  extern __shared__ __attribute__((aligned(16))) char lds[];
  const int wave = __builtin_amdgcn_readfirstlane(threadIdx.x >> 6);
  const int ngrp = (B + 3) >> 2;
  int grp = blockIdx.x * 4 + wave;
  const bool live = grp < ngrp;
  if (!live) grp = ngrp - 1;
  int sid0 = grp * 4;
  if (sid0 + 4 > B) sid0 = B - 4;
  const uint32_t own = live ? (0x1111u << (4 * (grp * 4 - sid0))) & 0x1111u : 0u;
  const int wb = WAVE0 + wave * WS;
  uint32_t okm, pend, pk = 0, done = 0, resx, keep = 0, livem = own;
  int c0, kf = 0x7fffffff, istr = F * 4, cur = -1;
  const int* idxp = idx + (size_t)sid0 * F;
  uint16_t* outp = out + sid0;
  for (;;) {
    asm volatile(
      "s_cmp_lg_u32 %[kf], 0x7fffffff\n\t"
      "s_cbranch_scc1 Lredo%=\n\t"
      "s_mov_b64 s[42:43], %[idx0]\n\t"
      "s_mov_b32 s36, 0x7fff7fff\n\t"
      "s_mov_b32 s37, 0x80008000\n\t"
      "s_add_u32 s58, s42, %[istr]\n\t"
      "s_addc_u32 s59, s43, 0\n\t"
      "s_lshl_b32 s34, %[wv], 6\n\t"
      "v_mbcnt_lo_u32_b32 v202, -1, 0\n\t"
      "v_mbcnt_hi_u32_b32 v202, -1, v202\n\t"
      "v_lshrrev_b32 v204, 4, v202\n\t"
      "v_and_b32 v203, 15, v202\n\t"
      "v_mul_u32_u24 v226, 28, v204\n\t"
      "v_min_u32 v226, 80, v226\n\t"
      "v_and_b32 v206, 1, v204\n\t"
      "v_mad_u32_u24 v227, v206, 28, 24\n\t"
      "global_load_dwordx4 v[164:167], v226, s[42:43] offset:0\n\t"
      "global_load_dwordx2 v[168:169], v226, s[42:43] offset:16\n\t"
      "global_load_dword v170, v227, s[42:43] offset:0\n\t"
      "v_lshlrev_b32 v208, 11, v204\n\t"
      "v_lshl_add_u32 v208, v203, 4, v208\n\t"
      "s_lshl_b32 s70, %[wv], 13\n\t"
      "v_add_u32 v208, s70, v208\n\t"
      "global_load_dwordx4 v[40:43], v208, %[W] offset:0\n\t"
      "s_mov_b64 s[40:41], %[tab]\n\t"
      "global_load_dwordx4 v[44:47], v208, %[W] offset:256\n\t"
      "global_load_dwordx4 v[48:51], v208, %[W] offset:512\n\t"
      "s_mov_b32 s49, %[wb]\n\t"
      "global_load_dwordx4 v[52:55], v208, %[W] offset:768\n\t"
      "global_load_dwordx4 v[56:59], v208, %[W] offset:1024\n\t"
      "v_lshlrev_b32 v228, 4, v203\n\t"
      "global_load_dwordx4 v[60:63], v208, %[W] offset:1280\n\t"
      "s_movk_i32 s61, 288\n\t"
      "global_load_dwordx4 v[64:67], v208, %[W] offset:1536\n\t"
      "global_load_dwordx4 v[68:71], v208, %[W] offset:1792\n\t"
      "v_lshl_add_u32 v205, v204, 4, s49\n\t"
      "global_load_dwordx3 v[172:174], v226, s[58:59]\n\t"
      "v_lshrrev_b32 v8, 2, v203\n\t"
      "v_mad_u32_u24 v212, v8, s61, v205\n\t"
      "v_and_b32 v8, 3, v203\n\t"
      "v_lshlrev_b32 v210, 2, v202\n\t"
      "global_load_dword v180, v210, %[b]\n\t"
      "v_mad_u32_u24 v213, v8, s61, v205\n\t"
      "v_and_b32 v8, 7, v203\n\t"
      "v_mad_u32_u24 v214, v8, s61, v205\n\t"
      "global_load_dword v181, v210, %[h]\n\t"
      "v_lshrrev_b32 v8, 3, v203\n\t"
      "v_add_u32 v8, 24, v8\n\t"
      "v_mad_u32_u24 v215, v8, s61, v205\n\t"
      "global_load_dword v182, v210, %[p]\n\t"
      "v_mad_u32_u24 v216, v203, s61, v205\n\t"
      "v_add_u32 v8, 16, v203\n\t"
      "v_min_u32 v8, 25, v8\n\t"
      "v_add_u32 v211, s34, v202\n\t"
      "v_lshlrev_b32 v211, 1, v211\n\t"
      "v_min_u32 v211, 323, v211\n\t"
      "v_lshlrev_b32 v211, 2, v211\n\t"
      "global_load_dwordx2 v[184:185], v211, %[ii]\n\t"
      "v_mad_u32_u24 v217, v8, s61, v205\n\t"
      "s_add_u32 s54, s49, 24896\n\t"
      "v_lshl_add_u32 v218, v202, 2, s54\n\t"
      "global_load_dwordx2 v[186:187], v211, %[jj]\n\t"
      "v_mov_b32 v219, v218\n\t"
      "v_add_u32 v9, s49, v228\n\t"
      "v_mad_u32_u24 v220, v204, s61, v9\n\t"
      "global_load_dwordx2 v[194:195], v211, %[ptab]\n\t"
      "v_and_b32 v8, 1, v204\n\t"
      "v_add_u32 v8, 24, v8\n\t"
      "v_mad_u32_u24 v221, v8, s61, v9\n\t"
      "v_lshlrev_b32 v210, 5, v202\n\t"
      "global_load_dwordx4 v[104:107], v210, %[ltab]\n\t"
      "v_add_u32 v222, 14976, v220\n\t"
      "v_add_u32 v223, 9216, v221\n\t"
      "v_mul_u32_u24 v8, 80, v203\n\t"
      "global_load_dwordx3 v[108:110], v210, %[ltab] offset:16\n\t"
      "v_add_u32 v8, 26176, v8\n\t"
      "v_add_u32 v224, v8, v205\n\t"
      "v_lshlrev_b32 v8, 4, v204\n\t"
      "s_waitcnt vmcnt(17)\n\t"
      "v_lshl_add_u32 v164, v164, 8, v228\n\t"
      "global_load_dwordx4 v[136:139], v164, s[40:41]\n\t"
      "v_add_u32 v225, 37760, v8\n\t"
      "v_mad_u32_u24 v229, v203, s61, v8\n\t"
      "v_mul_u32_u24 v207, 2304, v203\n\t"
      "v_lshrrev_b32 v206, 1, v203\n\t"
      "v_lshl_add_u32 v165, v165, 8, v228\n\t"
      "global_load_dwordx4 v[140:143], v165, s[40:41]\n\t"
      "v_add_u32 v206, v206, v204\n\t"
      "v_lshl_add_u32 v209, v206, 4, v207\n\t"
      "v_add_u32 v209, s34, v209\n\t"
      "v_mov_b32 v9, 0x3c003c00\n\t"
      "v_lshl_add_u32 v166, v166, 8, v228\n\t"
      "global_load_dwordx4 v[144:147], v166, s[40:41]\n\t"
      "v_cmp_eq_u32 vcc, 0, v203\n\t"
      "s_nop 1\n\t"
      "v_cndmask_b32 v8, 0, v9, vcc\n\t"
      "v_accvgpr_write_b32 a128, v8\n\t"
      "v_lshl_add_u32 v167, v167, 8, v228\n\t"
      "global_load_dwordx4 v[148:151], v167, s[40:41]\n\t"
      "v_accvgpr_write_b32 a129, v8\n\t"
      "v_accvgpr_write_b32 a130, v8\n\t"
      "v_accvgpr_write_b32 a131, v8\n\t"
      "v_cmp_eq_u32 vcc, 4, v203\n\t"
      "v_lshl_add_u32 v168, v168, 8, v228\n\t"
      "global_load_dwordx4 v[152:155], v168, s[40:41]\n\t"
      "s_nop 1\n\t"
      "v_cndmask_b32 v8, 0, v9, vcc\n\t"
      "v_accvgpr_write_b32 a132, v8\n\t"
      "v_accvgpr_write_b32 a133, v8\n\t"
      "v_lshl_add_u32 v169, v169, 8, v228\n\t"
      "global_load_dwordx4 v[156:159], v169, s[40:41]\n\t"
      "v_accvgpr_write_b32 a134, v8\n\t"
      "v_accvgpr_write_b32 a135, v8\n\t"
      "v_cmp_eq_u32 vcc, 8, v203\n\t"
      "s_nop 1\n\t"
      "v_lshl_add_u32 v170, v170, 8, v228\n\t"
      "global_load_dwordx4 v[160:163], v170, s[40:41]\n\t"
      "v_cndmask_b32 v8, 0, v9, vcc\n\t"
      "v_accvgpr_write_b32 a136, v8\n\t"
      "v_accvgpr_write_b32 a137, v8\n\t"
      "v_accvgpr_write_b32 a138, v8\n\t"
      "global_load_dwordx3 v[252:254], v226, s[58:59] offset:12\n\t"
      "v_accvgpr_write_b32 a139, v8\n\t"
      "v_cmp_eq_u32 vcc, 12, v203\n\t"
      "s_nop 1\n\t"
      "v_cndmask_b32 v8, 0, v9, vcc\n\t"
      "global_load_dword v251, v227, s[58:59]\n\t"
      "v_accvgpr_write_b32 a140, v8\n\t"
      "v_accvgpr_write_b32 a141, v8\n\t"
      "v_accvgpr_write_b32 a142, v8\n\t"
      "v_accvgpr_write_b32 a143, v8\n\t"
      "s_waitcnt vmcnt(18)\n\t"
      "v_lshlrev_b32 v8, 16, v40\n\t"
      "v_lshlrev_b32 v9, 16, v44\n\t"
      "v_and_b32 v10, 0xffff0000, v40\n\t"
      "v_and_b32 v11, 0xffff0000, v44\n\t"
      "v_max_f32 v16, |v8|, |v9|\n\t"
      "v_max3_f32 v16, v16, |v10|, |v11|\n\t"
      "v_cvt_pk_f16_f32 v72, v8, v9\n\t"
      "v_cvt_pk_f16_f32 v76, v10, v11\n\t"
      "v_lshlrev_b32 v12, 16, v48\n\t"
      "v_lshlrev_b32 v13, 16, v52\n\t"
      "v_and_b32 v14, 0xffff0000, v48\n\t"
      "v_and_b32 v15, 0xffff0000, v52\n\t"
      "v_max3_f32 v16, v16, |v12|, |v13|\n\t"
      "v_max3_f32 v16, v16, |v14|, |v15|\n\t"
      "v_cvt_pk_f16_f32 v73, v12, v13\n\t"
      "v_cvt_pk_f16_f32 v77, v14, v15\n\t"
      "v_lshlrev_b32 v8, 16, v56\n\t"
      "v_lshlrev_b32 v9, 16, v60\n\t"
      "v_and_b32 v10, 0xffff0000, v56\n\t"
      "v_and_b32 v11, 0xffff0000, v60\n\t"
      "v_max3_f32 v16, v16, |v8|, |v9|\n\t"
      "v_max3_f32 v16, v16, |v10|, |v11|\n\t"
      "v_cvt_pk_f16_f32 v74, v8, v9\n\t"
      "v_cvt_pk_f16_f32 v78, v10, v11\n\t"
      "v_lshlrev_b32 v12, 16, v64\n\t"
      "v_lshlrev_b32 v13, 16, v68\n\t"
      "v_and_b32 v14, 0xffff0000, v64\n\t"
      "v_and_b32 v15, 0xffff0000, v68\n\t"
      "v_max3_f32 v16, v16, |v12|, |v13|\n\t"
      "v_max3_f32 v16, v16, |v14|, |v15|\n\t"
      "v_cvt_pk_f16_f32 v75, v12, v13\n\t"
      "v_cvt_pk_f16_f32 v79, v14, v15\n\t"
      "v_lshlrev_b32 v8, 16, v41\n\t"
      "v_lshlrev_b32 v9, 16, v45\n\t"
      "v_and_b32 v10, 0xffff0000, v41\n\t"
      "v_and_b32 v11, 0xffff0000, v45\n\t"
      "v_max3_f32 v16, v16, |v8|, |v9|\n\t"
      "v_max3_f32 v16, v16, |v10|, |v11|\n\t"
      "v_cvt_pk_f16_f32 v80, v8, v9\n\t"
      "v_cvt_pk_f16_f32 v84, v10, v11\n\t"
      "ds_write_b128 v209, v[72:75] offset:0\n\t"
      "v_lshlrev_b32 v12, 16, v49\n\t"
      "v_lshlrev_b32 v13, 16, v53\n\t"
      "v_and_b32 v14, 0xffff0000, v49\n\t"
      "v_and_b32 v15, 0xffff0000, v53\n\t"
      "v_max3_f32 v16, v16, |v12|, |v13|\n\t"
      "v_max3_f32 v16, v16, |v14|, |v15|\n\t"
      "v_cvt_pk_f16_f32 v81, v12, v13\n\t"
      "v_cvt_pk_f16_f32 v85, v14, v15\n\t"
      "v_lshlrev_b32 v8, 16, v57\n\t"
      "v_lshlrev_b32 v9, 16, v61\n\t"
      "v_and_b32 v10, 0xffff0000, v57\n\t"
      "v_and_b32 v11, 0xffff0000, v61\n\t"
      "v_max3_f32 v16, v16, |v8|, |v9|\n\t"
      "v_max3_f32 v16, v16, |v10|, |v11|\n\t"
      "v_cvt_pk_f16_f32 v82, v8, v9\n\t"
      "v_cvt_pk_f16_f32 v86, v10, v11\n\t"
      "ds_write_b128 v209, v[76:79] offset:288\n\t"
      "v_lshlrev_b32 v12, 16, v65\n\t"
      "v_lshlrev_b32 v13, 16, v69\n\t"
      "v_and_b32 v14, 0xffff0000, v65\n\t"
      "v_and_b32 v15, 0xffff0000, v69\n\t"
      "v_max3_f32 v16, v16, |v12|, |v13|\n\t"
      "v_max3_f32 v16, v16, |v14|, |v15|\n\t"
      "v_cvt_pk_f16_f32 v83, v12, v13\n\t"
      "v_cvt_pk_f16_f32 v87, v14, v15\n\t"
      "v_lshlrev_b32 v8, 16, v42\n\t"
      "v_lshlrev_b32 v9, 16, v46\n\t"
      "v_and_b32 v10, 0xffff0000, v42\n\t"
      "v_and_b32 v11, 0xffff0000, v46\n\t"
      "v_max3_f32 v16, v16, |v8|, |v9|\n\t"
      "v_max3_f32 v16, v16, |v10|, |v11|\n\t"
      "v_cvt_pk_f16_f32 v88, v8, v9\n\t"
      "v_cvt_pk_f16_f32 v92, v10, v11\n\t"
      "ds_write_b128 v209, v[80:83] offset:576\n\t"
      "v_lshlrev_b32 v12, 16, v50\n\t"
      "v_lshlrev_b32 v13, 16, v54\n\t"
      "v_and_b32 v14, 0xffff0000, v50\n\t"
      "v_and_b32 v15, 0xffff0000, v54\n\t"
      "v_max3_f32 v16, v16, |v12|, |v13|\n\t"
      "v_max3_f32 v16, v16, |v14|, |v15|\n\t"
      "v_cvt_pk_f16_f32 v89, v12, v13\n\t"
      "v_cvt_pk_f16_f32 v93, v14, v15\n\t"
      "v_lshlrev_b32 v8, 16, v58\n\t"
      "v_lshlrev_b32 v9, 16, v62\n\t"
      "v_and_b32 v10, 0xffff0000, v58\n\t"
      "v_and_b32 v11, 0xffff0000, v62\n\t"
      "v_max3_f32 v16, v16, |v8|, |v9|\n\t"
      "v_max3_f32 v16, v16, |v10|, |v11|\n\t"
      "v_cvt_pk_f16_f32 v90, v8, v9\n\t"
      "v_cvt_pk_f16_f32 v94, v10, v11\n\t"
      "ds_write_b128 v209, v[84:87] offset:864\n\t"
      "v_lshlrev_b32 v12, 16, v66\n\t"
      "v_lshlrev_b32 v13, 16, v70\n\t"
      "v_and_b32 v14, 0xffff0000, v66\n\t"
      "v_and_b32 v15, 0xffff0000, v70\n\t"
      "v_max3_f32 v16, v16, |v12|, |v13|\n\t"
      "v_max3_f32 v16, v16, |v14|, |v15|\n\t"
      "v_cvt_pk_f16_f32 v91, v12, v13\n\t"
      "v_cvt_pk_f16_f32 v95, v14, v15\n\t"
      "v_lshlrev_b32 v8, 16, v43\n\t"
      "v_lshlrev_b32 v9, 16, v47\n\t"
      "v_and_b32 v10, 0xffff0000, v43\n\t"
      "v_and_b32 v11, 0xffff0000, v47\n\t"
      "v_max3_f32 v16, v16, |v8|, |v9|\n\t"
      "v_max3_f32 v16, v16, |v10|, |v11|\n\t"
      "v_cvt_pk_f16_f32 v96, v8, v9\n\t"
      "v_cvt_pk_f16_f32 v100, v10, v11\n\t"
      "ds_write_b128 v209, v[88:91] offset:1152\n\t"
      "v_lshlrev_b32 v12, 16, v51\n\t"
      "v_lshlrev_b32 v13, 16, v55\n\t"
      "v_and_b32 v14, 0xffff0000, v51\n\t"
      "v_and_b32 v15, 0xffff0000, v55\n\t"
      "v_max3_f32 v16, v16, |v12|, |v13|\n\t"
      "v_max3_f32 v16, v16, |v14|, |v15|\n\t"
      "v_cvt_pk_f16_f32 v97, v12, v13\n\t"
      "v_cvt_pk_f16_f32 v101, v14, v15\n\t"
      "v_lshlrev_b32 v8, 16, v59\n\t"
      "v_lshlrev_b32 v9, 16, v63\n\t"
      "v_and_b32 v10, 0xffff0000, v59\n\t"
      "v_and_b32 v11, 0xffff0000, v63\n\t"
      "v_max3_f32 v16, v16, |v8|, |v9|\n\t"
      "v_max3_f32 v16, v16, |v10|, |v11|\n\t"
      "v_cvt_pk_f16_f32 v98, v8, v9\n\t"
      "v_cvt_pk_f16_f32 v102, v10, v11\n\t"
      "ds_write_b128 v209, v[92:95] offset:1440\n\t"
      "v_lshlrev_b32 v12, 16, v67\n\t"
      "v_lshlrev_b32 v13, 16, v71\n\t"
      "v_and_b32 v14, 0xffff0000, v67\n\t"
      "v_and_b32 v15, 0xffff0000, v71\n\t"
      "v_max3_f32 v16, v16, |v12|, |v13|\n\t"
      "v_max3_f32 v16, v16, |v14|, |v15|\n\t"
      "v_cvt_pk_f16_f32 v99, v12, v13\n\t"
      "v_cvt_pk_f16_f32 v103, v14, v15\n\t"
      "s_waitcnt vmcnt(9)\n\t"
      "v_lshlrev_b32 v188, 16, v180\n\t"
      "v_and_b32 v189, 0xffff0000, v180\n\t"
      "v_max_f32 v17, |v188|, |v189|\n\t"
      "ds_write_b128 v209, v[96:99] offset:1728\n\t"
      "v_lshlrev_b32 v190, 16, v181\n\t"
      "v_and_b32 v191, 0xffff0000, v181\n\t"
      "v_max_f32 v18, |v190|, |v191|\n\t"
      "ds_write_b128 v209, v[100:103] offset:2016\n\t"
      "v_lshlrev_b32 v192, 16, v182\n\t"
      "v_and_b32 v193, 0xffff0000, v182\n\t"
      "v_max_f32 v19, |v192|, |v193|\n\t"
      "v_lshl_or_b32 v184, v186, 8, v184\n\t"
      "v_lshl_or_b32 v185, v187, 8, v185\n\t"
      "v_cmp_ne_u32 s[70:71], v184, v194\n\t"
      "v_cmp_ne_u32 vcc, v185, v195\n\t"
      "s_lshr_b32 s33, s34, 3\n\t"
      "s_add_u32 s33, s33, 38016\n\t"
      "v_max_u32_dpp v16, v16, v16 row_shr:1 row_mask:0xf bank_mask:0xf\n\t"
      "v_max_u32_dpp v17, v17, v17 row_shr:1 row_mask:0xf bank_mask:0xf\n\t"
      "v_max_u32_dpp v18, v18, v18 row_shr:1 row_mask:0xf bank_mask:0xf\n\t"
      "v_max_u32_dpp v19, v19, v19 row_shr:1 row_mask:0xf bank_mask:0xf\n\t"
      "s_or_b64 s[70:71], s[70:71], vcc\n\t"
      "s_or_b32 s67, s70, s71\n\t"
      "v_mul_f32 v188, 0x3a800000, v188\n\t"
      "v_max_u32_dpp v16, v16, v16 row_shr:2 row_mask:0xf bank_mask:0xf\n\t"
      "v_max_u32_dpp v17, v17, v17 row_shr:2 row_mask:0xf bank_mask:0xf\n\t"
      "v_max_u32_dpp v18, v18, v18 row_shr:2 row_mask:0xf bank_mask:0xf\n\t"
      "v_max_u32_dpp v19, v19, v19 row_shr:2 row_mask:0xf bank_mask:0xf\n\t"
      "v_mul_f32 v189, 0x3a800000, v189\n\t"
      "v_max_u32_dpp v16, v16, v16 row_shr:4 row_mask:0xf bank_mask:0xf\n\t"
      "v_max_u32_dpp v17, v17, v17 row_shr:4 row_mask:0xf bank_mask:0xf\n\t"
      "v_max_u32_dpp v18, v18, v18 row_shr:4 row_mask:0xf bank_mask:0xf\n\t"
      "v_max_u32_dpp v19, v19, v19 row_shr:4 row_mask:0xf bank_mask:0xf\n\t"
      "v_lshlrev_b32 v207, 3, v202\n\t"
      "v_max_u32_dpp v16, v16, v16 row_shr:8 row_mask:0xf bank_mask:0xf\n\t"
      "v_max_u32_dpp v17, v17, v17 row_shr:8 row_mask:0xf bank_mask:0xf\n\t"
      "v_max_u32_dpp v18, v18, v18 row_shr:8 row_mask:0xf bank_mask:0xf\n\t"
      "v_max_u32_dpp v19, v19, v19 row_shr:8 row_mask:0xf bank_mask:0xf\n\t"
      "ds_write_b64 v207, v[188:189] offset:36992\n\t"
      "v_max_u32_dpp v16, v16, v16 row_bcast:15 row_mask:0xa bank_mask:0xf\n\t"
      "v_max_u32_dpp v17, v17, v17 row_bcast:15 row_mask:0xa bank_mask:0xf\n\t"
      "v_max_u32_dpp v18, v18, v18 row_bcast:15 row_mask:0xa bank_mask:0xf\n\t"
      "v_max_u32_dpp v19, v19, v19 row_bcast:15 row_mask:0xa bank_mask:0xf\n\t"
      "v_lshlrev_b32 v208, 2, v202\n\t"
      "v_max_u32_dpp v16, v16, v16 row_bcast:31 row_mask:0xc bank_mask:0xf\n\t"
      "v_max_u32_dpp v17, v17, v17 row_bcast:31 row_mask:0xc bank_mask:0xf\n\t"
      "v_max_u32_dpp v18, v18, v18 row_bcast:31 row_mask:0xc bank_mask:0xf\n\t"
      "v_max_u32_dpp v19, v19, v19 row_bcast:31 row_mask:0xc bank_mask:0xf\n\t"
      "v_mov_b32 v206, s33\n\t"
      "v_mov_b32 v29, s67\n\t"
      "v_readlane_b32 s70, v16, 63\n\t"
      "v_readlane_b32 s72, v18, 63\n\t"
      "v_readlane_b32 s73, v19, 63\n\t"
      "v_readlane_b32 s71, v17, 63\n\t"
      "v_mov_b32 v28, s70\n\t"
      "ds_write_b64 v206, v[28:29]\n\t"
      "s_lshr_b32 s64, s72, 23\n\t"
      "s_max_u32 s64, s64, 1\n\t"
      "s_sub_i32 s64, s64, 126\n\t"
      "s_lshr_b32 s65, s73, 23\n\t"
      "s_max_u32 s65, s65, 1\n\t"
      "s_sub_i32 s65, s65, 126\n\t"
      "s_sub_i32 s64, 10, s64\n\t"
      "s_sub_i32 s65, 0, s65\n\t"
      "s_add_i32 s75, s64, 127\n\t"
      "s_lshl_b32 s75, s75, 23\n\t"
      "s_add_i32 s76, s65, 127\n\t"
      "s_lshl_b32 s76, s76, 23\n\t"
      "v_mul_f32 v190, s75, v190\n\t"
      "v_mul_f32 v191, s75, v191\n\t"
      "v_mul_f32 v192, s76, v192\n\t"
      "v_mul_f32 v193, s76, v193\n\t"
      "v_cvt_pk_f16_f32 v190, v190, v191\n\t"
      "v_cvt_pk_f16_f32 v192, v192, v193\n\t"
      "ds_write_b32 v208, v190 offset:37504\n\t"
      "ds_write_b32 v208, v192 offset:37760\n\t"
      "s_lshr_b32 s74, s71, 23\n\t"
      "s_max_u32 s74, s74, 1\n\t"
      "s_sub_i32 s74, s74, 126\n\t"
      "s_sub_i32 s79, -1, s74\n\t"
      "s_ashr_i32 s79, s79, 1\n\t"
      "v_lshlrev_b32 v207, 4, v204\n\t"
      "v_lshlrev_b32 v208, 3, v204\n\t"
      "v_mov_b32 v206, 38016\n\t"
      "s_waitcnt lgkmcnt(0)\n\t"
      "s_barrier\n\t"
      "Lpost%=:\n\t"
      "ds_read_b128 v[20:23], v206\n\t"
      "ds_read_b128 v[24:27], v206 offset:16\n\t"
      "ds_read_b128 a[0:3], v229 offset:0\n\t"
      "ds_read_b128 a[4:7], v229 offset:64\n\t"
      "ds_read_b128 a[8:11], v229 offset:128\n\t"
      "ds_read_b128 a[12:15], v229 offset:192\n\t"
      "ds_read_b128 a[16:19], v229 offset:4624\n\t"
      "ds_read_b128 a[20:23], v229 offset:4688\n\t"
      "ds_read_b128 a[24:27], v229 offset:4752\n\t"
      "ds_read_b128 a[28:31], v229 offset:4816\n\t"
      "s_waitcnt lgkmcnt(8)\n\t"
      "v_max3_u32 v20, v20, v22, v24\n\t"
      "v_or3_b32 v21, v21, v23, v25\n\t"
      "v_max_u32 v20, v20, v26\n\t"
      "v_or_b32 v21, v21, v27\n\t"
      "ds_read_b128 a[32:35], v229 offset:9248\n\t"
      "v_readfirstlane_b32 s70, v20\n\t"
      "v_readfirstlane_b32 s67, v21\n\t"
      "ds_read_b128 a[36:39], v229 offset:9312\n\t"
      "s_lshr_b32 s66, s70, 23\n\t"
      "s_max_u32 s66, s66, 1\n\t"
      "s_sub_i32 s66, s66, 126\n\t"
      "s_add_i32 s77, s66, 1\n\t"
      "s_ashr_i32 s77, s77, 1\n\t"
      "s_sub_i32 s63, 118, s77\n\t"
      "v_mov_b32 v210, s49\n\t"
      "v_add_u32_sdwa v230, v210, v104 dst_sel:DWORD dst_unused:UNUSED_PAD src0_sel:DWORD src1_sel:WORD_0\n\t"
      "v_add_u32_sdwa v231, v210, v104 dst_sel:DWORD dst_unused:UNUSED_PAD src0_sel:DWORD src1_sel:WORD_1\n\t"
      "ds_read_b128 a[40:43], v229 offset:9376\n\t"
      "v_add_u32_sdwa v232, v210, v105 dst_sel:DWORD dst_unused:UNUSED_PAD src0_sel:DWORD src1_sel:WORD_0\n\t"
      "v_add_u32_sdwa v233, v210, v105 dst_sel:DWORD dst_unused:UNUSED_PAD src0_sel:DWORD src1_sel:WORD_1\n\t"
      "ds_read_b128 a[44:47], v229 offset:9440\n\t"
      "v_add_u32_sdwa v234, v210, v106 dst_sel:DWORD dst_unused:UNUSED_PAD src0_sel:DWORD src1_sel:WORD_0\n\t"
      "v_add_u32_sdwa v235, v210, v106 dst_sel:DWORD dst_unused:UNUSED_PAD src0_sel:DWORD src1_sel:WORD_1\n\t"
      "ds_read_b128 a[48:51], v229 offset:13872\n\t"
      "v_add_u32_sdwa v236, v210, v107 dst_sel:DWORD dst_unused:UNUSED_PAD src0_sel:DWORD src1_sel:WORD_0\n\t"
      "v_add_u32_sdwa v237, v210, v107 dst_sel:DWORD dst_unused:UNUSED_PAD src0_sel:DWORD src1_sel:WORD_1\n\t"
      "ds_read_b128 a[52:55], v229 offset:13936\n\t"
      "v_add_u32_sdwa v238, v210, v108 dst_sel:DWORD dst_unused:UNUSED_PAD src0_sel:DWORD src1_sel:WORD_0\n\t"
      "v_add_u32_sdwa v239, v210, v108 dst_sel:DWORD dst_unused:UNUSED_PAD src0_sel:DWORD src1_sel:WORD_1\n\t"
      "ds_read_b128 a[56:59], v229 offset:14000\n\t"
      "v_add_u32_sdwa v240, v210, v109 dst_sel:DWORD dst_unused:UNUSED_PAD src0_sel:DWORD src1_sel:WORD_0\n\t"
      "v_add_u32_sdwa v241, v210, v109 dst_sel:DWORD dst_unused:UNUSED_PAD src0_sel:DWORD src1_sel:WORD_1\n\t"
      "ds_read_b128 a[60:63], v229 offset:14064\n\t"
      "v_add_u32_sdwa v242, v210, v110 dst_sel:DWORD dst_unused:UNUSED_PAD src0_sel:DWORD src1_sel:WORD_0\n\t"
      "s_waitcnt vmcnt(7)\n\t"
      "v_and_b32 v40, s36, v136\n\t"
      "v_and_b32 v41, s36, v137\n\t"
      "v_pk_max_u16 v52, v40, v41\n\t"
      "ds_read_b128 a[64:67], v229 offset:18496\n\t"
      "v_and_b32 v42, s36, v138\n\t"
      "v_and_b32 v43, s36, v139\n\t"
      "v_pk_maximum3_f16 v52, v52, v42, v43\n\t"
      "ds_read_b128 a[68:71], v229 offset:18560\n\t"
      "v_and_b32 v44, s36, v140\n\t"
      "v_and_b32 v45, s36, v141\n\t"
      "v_pk_max_u16 v53, v44, v45\n\t"
      "ds_read_b128 a[72:75], v229 offset:18624\n\t"
      "v_and_b32 v46, s36, v142\n\t"
      "v_and_b32 v47, s36, v143\n\t"
      "v_pk_maximum3_f16 v53, v53, v46, v47\n\t"
      "ds_read_b128 a[76:79], v229 offset:18688\n\t"
      "v_pk_max_u16 v243, v52, v53\n\t"
      "v_lshrrev_b32 v206, 16, v52\n\t"
      "v_lshrrev_b32 v210, 16, v53\n\t"
      "v_and_b32 v52, 0xffff, v52\n\t"
      "v_and_b32 v53, 0xffff, v53\n\t"
      "v_max_u32 v52, v52, v206\n\t"
      "v_max_u32 v53, v53, v210\n\t"
      "s_nop 0\n\t"
      "v_max_u32_dpp v52, v52, v52 row_shr:1 row_mask:0xf bank_mask:0xf\n\t"
      "v_max_u32_dpp v53, v53, v53 row_shr:1 row_mask:0xf bank_mask:0xf\n\t"
      "ds_read_b128 a[80:83], v229 offset:23120\n\t"
      "ds_read_b128 a[84:87], v229 offset:23184\n\t"
      "v_max_u32_dpp v52, v52, v52 row_shr:2 row_mask:0xf bank_mask:0xf\n\t"
      "v_max_u32_dpp v53, v53, v53 row_shr:2 row_mask:0xf bank_mask:0xf\n\t"
      "ds_read_b128 a[88:91], v229 offset:23248\n\t"
      "ds_read_b128 a[92:95], v229 offset:23312\n\t"
      "v_max_u32_dpp v52, v52, v52 row_shr:4 row_mask:0xf bank_mask:0xf\n\t"
      "v_max_u32_dpp v53, v53, v53 row_shr:4 row_mask:0xf bank_mask:0xf\n\t"
      "ds_read_b128 a[96:99], v229 offset:27744\n\t"
      "ds_read_b128 a[100:103], v229 offset:27808\n\t"
      "v_max_u32_dpp v52, v52, v52 row_shr:8 row_mask:0xf bank_mask:0xf\n\t"
      "v_max_u32_dpp v53, v53, v53 row_shr:8 row_mask:0xf bank_mask:0xf\n\t"
      "ds_read_b128 a[104:107], v229 offset:27872\n\t"
      "ds_read_b128 a[108:111], v229 offset:27936\n\t"
      "v_max_u32 v210, v52, v53\n\t"
      "v_lshrrev_b32 v52, 7, v52\n\t"
      "v_lshrrev_b32 v53, 7, v53\n\t"
      "v_add_u32 v52, -1, v52\n\t"
      "v_add_u32 v53, -1, v53\n\t"
      "v_min_u32 v52, 0xfe, v52\n\t"
      "v_min_u32 v53, 0xfe, v53\n\t"
      "v_add_u32 v54, v52, v53\n\t"
      "ds_read_b128 a[112:115], v229 offset:32368\n\t"
      "v_max_u32_dpp v210, v210, v210 row_bcast:15 row_mask:0xa bank_mask:0xf\n\t"
      "v_add_u32_dpp v54, v54, v54 row_bcast:15 row_mask:0xa bank_mask:0xf\n\t"
      "ds_read_b128 a[116:119], v229 offset:32432\n\t"
      "ds_read_b128 a[120:123], v229 offset:32496\n\t"
      "v_max_u32_dpp v210, v210, v210 row_bcast:31 row_mask:0xc bank_mask:0xf\n\t"
      "v_add_u32_dpp v54, v54, v54 row_bcast:31 row_mask:0xc bank_mask:0xf\n\t"
      "ds_read_b128 a[124:127], v229 offset:32560\n\t"
      "ds_read_b128 v[8:11], v207 offset:36992\n\t"
      "v_readlane_b32 s68, v210, 63\n\t"
      "v_readlane_b32 s75, v54, 63\n\t"
      "ds_read_b128 v[12:15], v207 offset:37056\n\t"
      "s_add_u32 s75, s75, 40\n\t"
      "s_lshr_b32 s76, s68, 7\n\t"
      "s_add_i32 s78, s63, 3\n\t"
      "s_sub_i32 s78, s78, %[kf]\n\t"
      "s_cmp_lg_u32 %[kf], 0x7fffffff\n\t"
      "s_cselect_b32 s76, s78, s76\n\t"
      "s_lshl_b32 s76, s76, 3\n\t"
      "s_cmp_lt_u32 s75, s76\n\t"
      "s_cselect_b32 s75, 1, 0\n\t"
      "s_or_b32 s67, s67, s75\n\t"
      "s_lshr_b32 s62, s68, 7\n\t"
      "ds_read_b128 v[16:19], v207 offset:37120\n\t"
      "s_max_u32 s62, s62, 1\n\t"
      "ds_read_b128 v[20:23], v207 offset:37184\n\t"
      "s_sub_i32 s62, 121, s62\n\t"
      "ds_read_b128 v[24:27], v207 offset:37248\n\t"
      "s_sub_i32 s62, s62, s77\n\t"
      "ds_read_b128 v[28:31], v207 offset:37312\n\t"
      "s_cmp_lg_u32 %[kf], 0x7fffffff\n\t"
      "ds_read_b128 v[32:35], v207 offset:37376\n\t"
      "s_cselect_b32 s62, %[kf], s62\n\t"
      "ds_read_b128 v[36:39], v207 offset:37440\n\t"
      "s_min_i32 s62, s62, s79\n\t"
      "s_min_i32 s62, s62, 40\n\t"
      "s_max_i32 s62, s62, -60\n\t"
      "s_sub_i32 s44, 112, s62\n\t"
      "s_lshl_b32 s44, s44, 7\n\t"
      "s_mul_i32 s44, s44, 0x10001\n\t"
      "v_pk_sub_u16 v40, v40, s44 clamp\n\t"
      "v_and_b32 v136, s37, v136\n\t"
      "v_lshl_or_b32 v136, v40, 3, v136\n\t"
      "v_pk_sub_u16 v41, v41, s44 clamp\n\t"
      "v_and_b32 v137, s37, v137\n\t"
      "v_lshl_or_b32 v137, v41, 3, v137\n\t"
      "v_pk_sub_u16 v42, v42, s44 clamp\n\t"
      "v_and_b32 v138, s37, v138\n\t"
      "v_lshl_or_b32 v138, v42, 3, v138\n\t"
      "v_pk_sub_u16 v43, v43, s44 clamp\n\t"
      "v_and_b32 v139, s37, v139\n\t"
      "v_lshl_or_b32 v139, v43, 3, v139\n\t"
      "ds_write_b128 v220, v[136:139] offset:0\n\t"
      "v_pk_sub_u16 v44, v44, s44 clamp\n\t"
      "v_and_b32 v140, s37, v140\n\t"
      "v_lshl_or_b32 v140, v44, 3, v140\n\t"
      "v_pk_sub_u16 v45, v45, s44 clamp\n\t"
      "v_and_b32 v141, s37, v141\n\t"
      "v_lshl_or_b32 v141, v45, 3, v141\n\t"
      "v_pk_sub_u16 v46, v46, s44 clamp\n\t"
      "v_and_b32 v142, s37, v142\n\t"
      "v_lshl_or_b32 v142, v46, 3, v142\n\t"
      "v_pk_sub_u16 v47, v47, s44 clamp\n\t"
      "v_and_b32 v143, s37, v143\n\t"
      "v_lshl_or_b32 v143, v47, 3, v143\n\t"
      "ds_write_b128 v220, v[140:143] offset:1152\n\t"
      "s_waitcnt vmcnt(6)\n\t"
      "v_and_b32 v48, s36, v144\n\t"
      "v_and_b32 v49, s36, v145\n\t"
      "v_pk_maximum3_f16 v243, v243, v48, v49\n\t"
      "v_pk_sub_u16 v48, v48, s44 clamp\n\t"
      "v_and_b32 v144, s37, v144\n\t"
      "v_lshl_or_b32 v144, v48, 3, v144\n\t"
      "v_pk_sub_u16 v49, v49, s44 clamp\n\t"
      "v_and_b32 v145, s37, v145\n\t"
      "v_lshl_or_b32 v145, v49, 3, v145\n\t"
      "v_and_b32 v50, s36, v146\n\t"
      "v_and_b32 v51, s36, v147\n\t"
      "v_pk_maximum3_f16 v243, v243, v50, v51\n\t"
      "v_pk_sub_u16 v50, v50, s44 clamp\n\t"
      "v_and_b32 v146, s37, v146\n\t"
      "v_lshl_or_b32 v146, v50, 3, v146\n\t"
      "v_pk_sub_u16 v51, v51, s44 clamp\n\t"
      "v_and_b32 v147, s37, v147\n\t"
      "v_lshl_or_b32 v147, v51, 3, v147\n\t"
      "ds_write_b128 v220, v[144:147] offset:2304\n\t"
      "v_lshl_add_u32 v172, v172, 8, v228\n\t"
      "v_lshl_add_u32 v173, v173, 8, v228\n\t"
      "v_lshl_add_u32 v174, v174, 8, v228\n\t"
      "global_load_dwordx4 v[136:139], v172, s[40:41]\n\t"
      "global_load_dwordx4 v[140:143], v173, s[40:41]\n\t"
      "global_load_dwordx4 v[144:147], v174, s[40:41]\n\t"
      "s_add_u32 s42, s58, %[istr]\n\t"
      "s_addc_u32 s43, s59, 0\n\t"
      "global_load_dwordx3 v[248:250], v226, s[42:43]\n\t"
      "s_add_u32 s38, s42, %[istr]\n\t"
      "s_addc_u32 s39, s43, 0\n\t"
      "ds_read_b128 v[72:75], v213 offset:1152\n\t"
      "ds_read_b128 v[88:91], v212 offset:0\n\t"
      "ds_read_b128 v[76:79], v213 offset:1216\n\t"
      "ds_read_b128 v[92:95], v212 offset:64\n\t"
      "ds_read_b128 v[80:83], v213 offset:1280\n\t"
      "ds_read_b128 v[96:99], v212 offset:128\n\t"
      "ds_read_b128 v[84:87], v213 offset:1344\n\t"
      "ds_read_b128 v[100:103], v212 offset:192\n\t"
      "ds_read_b64 v[120:121], v208 offset:37504\n\t"
      "ds_read_b64 v[122:123], v208 offset:37536\n\t"
      "ds_read_b64 v[124:125], v208 offset:37568\n\t"
      "ds_read_b64 v[126:127], v208 offset:37600\n\t"
      "ds_read_b64 v[128:129], v208 offset:37632\n\t"
      "ds_read_b64 v[130:131], v208 offset:37664\n\t"
      "ds_read_b64 v[132:133], v208 offset:37696\n\t"
      "ds_read_b64 v[134:135], v208 offset:37728\n\t"
      "s_lshl_b32 s60, s62, 1\n\t"
      "s_add_i32 s70, s64, s60\n\t"
      "s_add_i32 s60, s60, 137\n\t"
      "s_lshl_b32 s60, s60, 23\n\t"
      "s_sub_i32 s69, 0, s70\n\t"
      "s_min_i32 s70, s69, 120\n\t"
      "s_max_i32 s70, s70, -120\n\t"
      "s_lshl_b32 s70, s70, 23\n\t"
      "s_add_u32 s70, s70, 0x3fb8aa3b\n\t"
      "v_mov_b32 v108, s70\n\t"
      "s_mov_b32 s45, 7488\n\t"
      "s_mov_b32 s46, 4294966016\n\t"
      "s_mov_b32 s48, 0\n\t"
      "s_mov_b64 s[50:51], 0\n\t"
      "s_mov_b32 s47, 0\n\t"
      "v_mov_b32 v201, 0\n\t"
      "s_mov_b32 s52, 0x1000f\n\t"
      "s_mov_b32 s53, 0\n\t"
      "s_mov_b32 m0, 60\n\t"
      "s_and_b32 s75, s64, 0xff\n\t"
      "s_and_b32 s76, s65, 0xff\n\t"
      "s_lshl_b32 s76, s76, 8\n\t"
      "s_or_b32 s75, s75, s76\n\t"
      "s_and_b32 s76, s79, 0xff\n\t"
      "s_lshl_b32 s76, s76, 16\n\t"
      "s_or_b32 s35, s75, s76\n\t"
      "s_cmp_eq_i32 s62, -5\n\t"
      "s_cbranch_scc1 Lnobias%=\n\t"
      "s_waitcnt lgkmcnt(15)\n\t"
      "v_mul_f32 v8, s60, v8\n\t"
      "v_mul_f32 v9, s60, v9\n\t"
      "v_mul_f32 v10, s60, v10\n\t"
      "v_mul_f32 v11, s60, v11\n\t"
      "v_mul_f32 v12, s60, v12\n\t"
      "v_mul_f32 v13, s60, v13\n\t"
      "v_mul_f32 v14, s60, v14\n\t"
      "v_mul_f32 v15, s60, v15\n\t"
      "v_mul_f32 v16, s60, v16\n\t"
      "v_mul_f32 v17, s60, v17\n\t"
      "v_mul_f32 v18, s60, v18\n\t"
      "v_mul_f32 v19, s60, v19\n\t"
      "v_mul_f32 v20, s60, v20\n\t"
      "v_mul_f32 v21, s60, v21\n\t"
      "v_mul_f32 v22, s60, v22\n\t"
      "v_mul_f32 v23, s60, v23\n\t"
      "v_mul_f32 v24, s60, v24\n\t"
      "v_mul_f32 v25, s60, v25\n\t"
      "v_mul_f32 v26, s60, v26\n\t"
      "v_mul_f32 v27, s60, v27\n\t"
      "v_mul_f32 v28, s60, v28\n\t"
      "v_mul_f32 v29, s60, v29\n\t"
      "v_mul_f32 v30, s60, v30\n\t"
      "v_mul_f32 v31, s60, v31\n\t"
      "v_mul_f32 v32, s60, v32\n\t"
      "v_mul_f32 v33, s60, v33\n\t"
      "v_mul_f32 v34, s60, v34\n\t"
      "v_mul_f32 v35, s60, v35\n\t"
      "v_mul_f32 v36, s60, v36\n\t"
      "v_mul_f32 v37, s60, v37\n\t"
      "v_mul_f32 v38, s60, v38\n\t"
      "v_mul_f32 v39, s60, v39\n\t"
      "Lnobias%=:\n\t"
      "s_waitcnt lgkmcnt(8)\n\t"
      "v_pk_mul_f16 v40, v72, v88\n\t"
      "v_pk_mul_f16 v41, v73, v89\n\t"
      "v_pk_mul_f16 v42, v74, v90\n\t"
      "v_pk_mul_f16 v43, v75, v91\n\t"
      "v_pk_mul_f16 v44, v76, v92\n\t"
      "v_pk_mul_f16 v45, v77, v93\n\t"
      "v_pk_mul_f16 v46, v78, v94\n\t"
      "v_pk_mul_f16 v47, v79, v95\n\t"
      "v_pk_mul_f16 v48, v80, v96\n\t"
      "v_pk_mul_f16 v49, v81, v97\n\t"
      "v_pk_mul_f16 v50, v82, v98\n\t"
      "v_pk_mul_f16 v51, v83, v99\n\t"
      "v_pk_mul_f16 v52, v84, v100\n\t"
      "v_pk_mul_f16 v53, v85, v101\n\t"
      "v_pk_mul_f16 v54, v86, v102\n\t"
      "v_pk_mul_f16 v55, v87, v103\n\t"
      "ds_read_b128 v[72:75], v213 offset:2304\n\t"
      "ds_read_b128 v[76:79], v213 offset:2368\n\t"
      "ds_read_b128 v[80:83], v213 offset:2432\n\t"
      "ds_read_b128 v[84:87], v213 offset:2496\n\t"
      "s_waitcnt lgkmcnt(0)\n\t"
      "Lloop%=:\n\t"
      "v_mfma_f32_16x16x32_f16 v[0:3], a[0:3], v[40:43], v[8:11]\n\t"
      "v_pk_fmac_f16 v112, v104, v132\n\t"
      "v_mfma_f32_16x16x32_f16 v[0:3], a[4:7], v[44:47], v[0:3]\n\t"
      "s_waitcnt lgkmcnt(4)\n\t"
      "v_pk_fmac_f16 v113, v105, v133\n\t"
      "v_pk_mul_f16 v56, v72, v88\n\t"
      "v_mfma_f32_16x16x32_f16 v[0:3], a[8:11], v[48:51], v[0:3]\n\t"
      "s_waitcnt vmcnt(9)\n\t"
      "v_cvt_pk_f16_f32 v104, v4, v5 clamp\n\t"
      "v_and_b32 v106, s36, v148\n\t"
      "v_mfma_f32_16x16x32_f16 v[0:3], a[12:15], v[52:55], v[0:3]\n\t"
      "v_cvt_pk_f16_f32 v105, v6, v7 clamp\n\t"
      "v_pk_mul_f16 v57, v73, v89\n\t"
      "v_mfma_f32_16x16x32_f16 v[4:7], a[16:19], v[40:43], v[12:15]\n\t"
      "v_pk_fmac_f16 v114, v104, v134\n\t"
      "v_and_b32 v107, s36, v149\n\t"
      "v_mfma_f32_16x16x32_f16 v[4:7], a[20:23], v[44:47], v[4:7]\n\t"
      "v_pk_fmac_f16 v115, v105, v135\n\t"
      "v_pk_mul_f16 v58, v74, v90\n\t"
      "v_mfma_f32_16x16x32_f16 v[4:7], a[24:27], v[48:51], v[4:7]\n\t"
      "v_cvt_pk_f16_f32 v104, v0, v1 clamp\n\t"
      "v_pk_maximum3_f16 v243, v243, v106, v107\n\t"
      "v_mfma_f32_16x16x32_f16 v[4:7], a[28:31], v[52:55], v[4:7]\n\t"
      "v_cvt_pk_f16_f32 v105, v2, v3 clamp\n\t"
      "v_pk_mul_f16 v59, v75, v91\n\t"
      "v_mfma_f32_16x16x32_f16 v[116:119], a[140:143], v[112:115], v[116:119]\n\t"
      "ds_read_b128 v[88:91], v212 offset:1152\n\t"
      "v_pk_sub_u16 v106, v106, s44 clamp\n\t"
      "v_mfma_f32_16x16x32_f16 v[0:3], a[32:35], v[40:43], v[16:19]\n\t"
      "v_pk_mul_f16 v112, v104, v120\n\t"
      "v_and_b32 v148, s37, v148\n\t"
      "v_mfma_f32_16x16x32_f16 v[0:3], a[36:39], v[44:47], v[0:3]\n\t"
      "v_pk_mul_f16 v113, v105, v121\n\t"
      "v_pk_mul_f16 v60, v76, v92\n\t"
      "v_mfma_f32_16x16x32_f16 v[0:3], a[40:43], v[48:51], v[0:3]\n\t"
      "v_cvt_pk_f16_f32 v104, v4, v5 clamp\n\t"
      "v_pk_sub_u16 v107, v107, s44 clamp\n\t"
      "v_mfma_f32_16x16x32_f16 v[0:3], a[44:47], v[52:55], v[0:3]\n\t"
      "v_cvt_pk_f16_f32 v105, v6, v7 clamp\n\t"
      "v_pk_mul_f16 v61, v77, v93\n\t"
      "v_mfma_f32_16x16x32_f16 v[4:7], a[48:51], v[40:43], v[20:23]\n\t"
      "ds_write_b32 v218, v116 offset:1024\n\t"
      "v_pk_mul_f16 v114, v104, v122\n\t"
      "v_mfma_f32_16x16x32_f16 v[4:7], a[52:55], v[44:47], v[4:7]\n\t"
      "v_pk_mul_f16 v115, v105, v123\n\t"
      "v_pk_mul_f16 v62, v78, v94\n\t"
      "v_mfma_f32_16x16x32_f16 v[4:7], a[56:59], v[48:51], v[4:7]\n\t"
      "v_cvt_pk_f16_f32 v104, v0, v1 clamp\n\t"
      "v_add_u32 v218, s46, v218\n\t"
      "v_mfma_f32_16x16x32_f16 v[4:7], a[60:63], v[52:55], v[4:7]\n\t"
      "v_cvt_pk_f16_f32 v105, v2, v3 clamp\n\t"
      "v_pk_mul_f16 v63, v79, v95\n\t"
      "v_mfma_f32_16x16x32_f16 v[0:3], a[64:67], v[40:43], v[24:27]\n\t"
      "ds_read_b128 v[92:95], v212 offset:1216\n\t"
      "v_pk_fmac_f16 v112, v104, v124\n\t"
      "v_mfma_f32_16x16x32_f16 v[0:3], a[68:71], v[44:47], v[0:3]\n\t"
      "s_waitcnt lgkmcnt(3)\n\t"
      "v_pk_fmac_f16 v113, v105, v125\n\t"
      "v_pk_mul_f16 v64, v80, v96\n\t"
      "v_mfma_f32_16x16x32_f16 v[0:3], a[72:75], v[48:51], v[0:3]\n\t"
      "v_cvt_pk_f16_f32 v104, v4, v5 clamp\n\t"
      "v_and_b32 v149, s37, v149\n\t"
      "v_mfma_f32_16x16x32_f16 v[0:3], a[76:79], v[52:55], v[0:3]\n\t"
      "v_cvt_pk_f16_f32 v105, v6, v7 clamp\n\t"
      "v_pk_mul_f16 v65, v81, v97\n\t"
      "v_mfma_f32_16x16x32_f16 v[4:7], a[80:83], v[40:43], v[28:31]\n\t"
      "v_pk_fmac_f16 v114, v104, v126\n\t"
      "v_lshl_or_b32 v148, v106, 3, v148\n\t"
      "v_mfma_f32_16x16x32_f16 v[4:7], a[84:87], v[44:47], v[4:7]\n\t"
      "v_pk_fmac_f16 v115, v105, v127\n\t"
      "v_pk_mul_f16 v66, v82, v98\n\t"
      "v_mfma_f32_16x16x32_f16 v[4:7], a[88:91], v[48:51], v[4:7]\n\t"
      "v_cvt_pk_f16_f32 v104, v0, v1 clamp\n\t"
      "v_lshl_or_b32 v149, v107, 3, v149\n\t"
      "v_mfma_f32_16x16x32_f16 v[4:7], a[92:95], v[52:55], v[4:7]\n\t"
      "v_cvt_pk_f16_f32 v105, v2, v3 clamp\n\t"
      "v_pk_mul_f16 v67, v83, v99\n\t"
      "v_mfma_f32_16x16x32_f16 v[0:3], a[96:99], v[40:43], v[32:35]\n\t"
      "ds_read_b128 v[96:99], v212 offset:1280\n\t"
      "v_pk_fmac_f16 v112, v104, v128\n\t"
      "v_mfma_f32_16x16x32_f16 v[0:3], a[100:103], v[44:47], v[0:3]\n\t"
      "v_pk_fmac_f16 v113, v105, v129\n\t"
      "v_pk_mul_f16 v68, v84, v100\n\t"
      "v_mfma_f32_16x16x32_f16 v[0:3], a[104:107], v[48:51], v[0:3]\n\t"
      "v_cvt_pk_f16_f32 v104, v4, v5 clamp\n\t"
      "v_and_b32 v106, s36, v150\n\t"
      "v_mfma_f32_16x16x32_f16 v[0:3], a[108:111], v[52:55], v[0:3]\n\t"
      "v_cvt_pk_f16_f32 v105, v6, v7 clamp\n\t"
      "v_pk_mul_f16 v69, v85, v101\n\t"
      "v_mfma_f32_16x16x32_f16 v[4:7], a[112:115], v[40:43], v[36:39]\n\t"
      "v_pk_fmac_f16 v114, v104, v130\n\t"
      "v_pk_mul_f16 v70, v86, v102\n\t"
      "v_mfma_f32_16x16x32_f16 v[4:7], a[116:119], v[44:47], v[4:7]\n\t"
      "v_pk_fmac_f16 v115, v105, v131\n\t"
      "v_pk_mul_f16 v71, v87, v103\n\t"
      "v_mfma_f32_16x16x32_f16 v[4:7], a[120:123], v[48:51], v[4:7]\n\t"
      "ds_read_b128 v[100:103], v212 offset:1344\n\t"
      "v_cvt_pk_f16_f32 v104, v0, v1 clamp\n\t"
      "v_mfma_f32_16x16x32_f16 v[4:7], a[124:127], v[52:55], v[4:7]\n\t"
      "v_cvt_pk_f16_f32 v105, v2, v3 clamp\n\t"
      "v_and_b32 v107, s36, v151\n\t"
      "v_mfma_f32_16x16x32_f16 v[0:3], a[0:3], v[56:59], v[8:11]\n\t"
      "v_pk_fmac_f16 v112, v104, v132\n\t"
      "v_pk_maximum3_f16 v243, v243, v106, v107\n\t"
      "v_mfma_f32_16x16x32_f16 v[0:3], a[4:7], v[60:63], v[0:3]\n\t"
      "s_waitcnt lgkmcnt(2)\n\t"
      "v_pk_fmac_f16 v113, v105, v133\n\t"
      "v_pk_mul_f16 v40, v72, v88\n\t"
      "v_mfma_f32_16x16x32_f16 v[0:3], a[8:11], v[64:67], v[0:3]\n\t"
      "v_cvt_pk_f16_f32 v104, v4, v5 clamp\n\t"
      "v_pk_sub_u16 v106, v106, s44 clamp\n\t"
      "v_mfma_f32_16x16x32_f16 v[0:3], a[12:15], v[68:71], v[0:3]\n\t"
      "v_cvt_pk_f16_f32 v105, v6, v7 clamp\n\t"
      "v_pk_mul_f16 v41, v73, v89\n\t"
      "v_mfma_f32_16x16x32_f16 v[4:7], a[16:19], v[56:59], v[12:15]\n\t"
      "v_pk_fmac_f16 v114, v104, v134\n\t"
      "v_and_b32 v150, s37, v150\n\t"
      "v_mfma_f32_16x16x32_f16 v[4:7], a[20:23], v[60:63], v[4:7]\n\t"
      "v_pk_fmac_f16 v115, v105, v135\n\t"
      "v_pk_mul_f16 v42, v74, v90\n\t"
      "v_mfma_f32_16x16x32_f16 v[4:7], a[24:27], v[64:67], v[4:7]\n\t"
      "v_cvt_pk_f16_f32 v104, v0, v1 clamp\n\t"
      "v_pk_sub_u16 v107, v107, s44 clamp\n\t"
      "v_mfma_f32_16x16x32_f16 v[4:7], a[28:31], v[68:71], v[4:7]\n\t"
      "v_cvt_pk_f16_f32 v105, v2, v3 clamp\n\t"
      "v_pk_mul_f16 v43, v75, v91\n\t"
      "v_mfma_f32_16x16x32_f16 v[116:119], a[128:131], v[112:115], 0\n\t"
      "ds_read_b128 v[88:91], v231 offset:0\n\t"
      "v_and_b32 v151, s37, v151\n\t"
      "v_mfma_f32_16x16x32_f16 v[0:3], a[32:35], v[56:59], v[16:19]\n\t"
      "ds_read_b128 v[72:75], v230 offset:0\n\t"
      "v_pk_mul_f16 v112, v104, v120\n\t"
      "v_mfma_f32_16x16x32_f16 v[0:3], a[36:39], v[60:63], v[0:3]\n\t"
      "v_pk_mul_f16 v113, v105, v121\n\t"
      "v_pk_mul_f16 v44, v76, v92\n\t"
      "v_mfma_f32_16x16x32_f16 v[0:3], a[40:43], v[64:67], v[0:3]\n\t"
      "v_cvt_pk_f16_f32 v104, v4, v5 clamp\n\t"
      "v_lshl_or_b32 v150, v106, 3, v150\n\t"
      "v_mfma_f32_16x16x32_f16 v[0:3], a[44:47], v[68:71], v[0:3]\n\t"
      "v_cvt_pk_f16_f32 v105, v6, v7 clamp\n\t"
      "v_pk_mul_f16 v45, v77, v93\n\t"
      "v_mfma_f32_16x16x32_f16 v[4:7], a[48:51], v[56:59], v[20:23]\n\t"
      "v_pk_mul_f16 v114, v104, v122\n\t"
      "v_lshl_or_b32 v151, v107, 3, v151\n\t"
      "v_mfma_f32_16x16x32_f16 v[4:7], a[52:55], v[60:63], v[4:7]\n\t"
      "v_pk_mul_f16 v115, v105, v123\n\t"
      "v_pk_mul_f16 v46, v78, v94\n\t"
      "v_mfma_f32_16x16x32_f16 v[4:7], a[56:59], v[64:67], v[4:7]\n\t"
      "s_waitcnt vmcnt(8)\n\t"
      "ds_write_b128 v220, v[148:151] offset:3456\n\t"
      "v_cvt_pk_f16_f32 v104, v0, v1 clamp\n\t"
      "v_and_b32 v106, s36, v152\n\t"
      "v_mfma_f32_16x16x32_f16 v[4:7], a[60:63], v[68:71], v[4:7]\n\t"
      "v_cvt_pk_f16_f32 v105, v2, v3 clamp\n\t"
      "v_pk_mul_f16 v47, v79, v95\n\t"
      "v_mfma_f32_16x16x32_f16 v[0:3], a[64:67], v[56:59], v[24:27]\n\t"
      "ds_read_b128 v[92:95], v231 offset:64\n\t"
      "v_pk_fmac_f16 v112, v104, v124\n\t"
      "v_mfma_f32_16x16x32_f16 v[0:3], a[68:71], v[60:63], v[0:3]\n\t"
      "s_waitcnt lgkmcnt(4)\n\t"
      "ds_read_b128 v[76:79], v230 offset:64\n\t"
      "v_pk_fmac_f16 v113, v105, v125\n\t"
      "v_pk_mul_f16 v48, v80, v96\n\t"
      "v_mfma_f32_16x16x32_f16 v[0:3], a[72:75], v[64:67], v[0:3]\n\t"
      "v_cvt_pk_f16_f32 v104, v4, v5 clamp\n\t"
      "v_and_b32 v107, s36, v153\n\t"
      "v_mfma_f32_16x16x32_f16 v[0:3], a[76:79], v[68:71], v[0:3]\n\t"
      "v_cvt_pk_f16_f32 v105, v6, v7 clamp\n\t"
      "v_pk_mul_f16 v49, v81, v97\n\t"
      "v_mfma_f32_16x16x32_f16 v[4:7], a[80:83], v[56:59], v[28:31]\n\t"
      "v_pk_fmac_f16 v114, v104, v126\n\t"
      "v_pk_maximum3_f16 v243, v243, v106, v107\n\t"
      "v_mfma_f32_16x16x32_f16 v[4:7], a[84:87], v[60:63], v[4:7]\n\t"
      "v_pk_fmac_f16 v115, v105, v127\n\t"
      "v_pk_mul_f16 v50, v82, v98\n\t"
      "v_mfma_f32_16x16x32_f16 v[4:7], a[88:91], v[64:67], v[4:7]\n\t"
      "v_cvt_pk_f16_f32 v104, v0, v1 clamp\n\t"
      "v_pk_sub_u16 v106, v106, s44 clamp\n\t"
      "v_mfma_f32_16x16x32_f16 v[4:7], a[92:95], v[68:71], v[4:7]\n\t"
      "v_cvt_pk_f16_f32 v105, v2, v3 clamp\n\t"
      "v_pk_mul_f16 v51, v83, v99\n\t"
      "v_mfma_f32_16x16x32_f16 v[0:3], a[96:99], v[56:59], v[32:35]\n\t"
      "ds_read_b128 v[96:99], v231 offset:128\n\t"
      "v_pk_fmac_f16 v112, v104, v128\n\t"
      "v_mfma_f32_16x16x32_f16 v[0:3], a[100:103], v[60:63], v[0:3]\n\t"
      "ds_read_b128 v[80:83], v230 offset:128\n\t"
      "v_pk_fmac_f16 v113, v105, v129\n\t"
      "v_pk_mul_f16 v52, v84, v100\n\t"
      "v_mfma_f32_16x16x32_f16 v[0:3], a[104:107], v[64:67], v[0:3]\n\t"
      "v_cvt_pk_f16_f32 v104, v4, v5 clamp\n\t"
      "v_and_b32 v152, s37, v152\n\t"
      "v_mfma_f32_16x16x32_f16 v[0:3], a[108:111], v[68:71], v[0:3]\n\t"
      "v_cvt_pk_f16_f32 v105, v6, v7 clamp\n\t"
      "v_pk_mul_f16 v53, v85, v101\n\t"
      "v_mfma_f32_16x16x32_f16 v[4:7], a[112:115], v[56:59], v[36:39]\n\t"
      "v_pk_fmac_f16 v114, v104, v130\n\t"
      "v_pk_mul_f16 v54, v86, v102\n\t"
      "v_mfma_f32_16x16x32_f16 v[4:7], a[116:119], v[60:63], v[4:7]\n\t"
      "v_pk_fmac_f16 v115, v105, v131\n\t"
      "v_pk_mul_f16 v55, v87, v103\n\t"
      "v_mfma_f32_16x16x32_f16 v[4:7], a[120:123], v[64:67], v[4:7]\n\t"
      "ds_read_b128 v[100:103], v231 offset:192\n\t"
      "v_cvt_pk_f16_f32 v104, v0, v1 clamp\n\t"
      "v_mfma_f32_16x16x32_f16 v[4:7], a[124:127], v[68:71], v[4:7]\n\t"
      "ds_read_b128 v[84:87], v230 offset:192\n\t"
      "v_cvt_pk_f16_f32 v105, v2, v3 clamp\n\t"
      "v_mfma_f32_16x16x32_f16 v[0:3], a[0:3], v[40:43], v[8:11]\n\t"
      "v_pk_fmac_f16 v112, v104, v132\n\t"
      "v_add_u32 v230, s45, v230\n\t"
      "v_mfma_f32_16x16x32_f16 v[0:3], a[4:7], v[44:47], v[0:3]\n\t"
      "s_waitcnt lgkmcnt(4)\n\t"
      "v_pk_fmac_f16 v113, v105, v133\n\t"
      "v_pk_mul_f16 v56, v72, v88\n\t"
      "v_mfma_f32_16x16x32_f16 v[0:3], a[8:11], v[48:51], v[0:3]\n\t"
      "v_cvt_pk_f16_f32 v104, v4, v5 clamp\n\t"
      "v_add_u32 v231, s45, v231\n\t"
      "v_mfma_f32_16x16x32_f16 v[0:3], a[12:15], v[52:55], v[0:3]\n\t"
      "v_cvt_pk_f16_f32 v105, v6, v7 clamp\n\t"
      "v_pk_mul_f16 v57, v73, v89\n\t"
      "v_mfma_f32_16x16x32_f16 v[4:7], a[16:19], v[40:43], v[12:15]\n\t"
      "v_pk_fmac_f16 v114, v104, v134\n\t"
      "v_pk_sub_u16 v107, v107, s44 clamp\n\t"
      "v_mfma_f32_16x16x32_f16 v[4:7], a[20:23], v[44:47], v[4:7]\n\t"
      "v_pk_fmac_f16 v115, v105, v135\n\t"
      "v_pk_mul_f16 v58, v74, v90\n\t"
      "v_mfma_f32_16x16x32_f16 v[4:7], a[24:27], v[48:51], v[4:7]\n\t"
      "v_cvt_pk_f16_f32 v104, v0, v1 clamp\n\t"
      "v_and_b32 v153, s37, v153\n\t"
      "v_mfma_f32_16x16x32_f16 v[4:7], a[28:31], v[52:55], v[4:7]\n\t"
      "v_cvt_pk_f16_f32 v105, v2, v3 clamp\n\t"
      "v_pk_mul_f16 v59, v75, v91\n\t"
      "v_mfma_f32_16x16x32_f16 v[116:119], a[132:135], v[112:115], v[116:119]\n\t"
      "ds_read_b128 v[88:91], v212 offset:0\n\t"
      "v_lshl_or_b32 v152, v106, 3, v152\n\t"
      "v_mfma_f32_16x16x32_f16 v[0:3], a[32:35], v[40:43], v[16:19]\n\t"
      "ds_read_b128 v[72:75], v213 offset:3456\n\t"
      "v_pk_mul_f16 v112, v104, v120\n\t"
      "v_mfma_f32_16x16x32_f16 v[0:3], a[36:39], v[44:47], v[0:3]\n\t"
      "v_pk_mul_f16 v113, v105, v121\n\t"
      "v_pk_mul_f16 v60, v76, v92\n\t"
      "v_mfma_f32_16x16x32_f16 v[0:3], a[40:43], v[48:51], v[0:3]\n\t"
      "v_cvt_pk_f16_f32 v104, v4, v5 clamp\n\t"
      "v_lshl_or_b32 v153, v107, 3, v153\n\t"
      "v_mfma_f32_16x16x32_f16 v[0:3], a[44:47], v[52:55], v[0:3]\n\t"
      "v_cvt_pk_f16_f32 v105, v6, v7 clamp\n\t"
      "v_pk_mul_f16 v61, v77, v93\n\t"
      "v_mfma_f32_16x16x32_f16 v[4:7], a[48:51], v[40:43], v[20:23]\n\t"
      "v_pk_mul_f16 v114, v104, v122\n\t"
      "v_and_b32 v106, s36, v154\n\t"
      "v_mfma_f32_16x16x32_f16 v[4:7], a[52:55], v[44:47], v[4:7]\n\t"
      "v_pk_mul_f16 v115, v105, v123\n\t"
      "v_pk_mul_f16 v62, v78, v94\n\t"
      "v_mfma_f32_16x16x32_f16 v[4:7], a[56:59], v[48:51], v[4:7]\n\t"
      "v_cvt_pk_f16_f32 v104, v0, v1 clamp\n\t"
      "v_and_b32 v107, s36, v155\n\t"
      "v_mfma_f32_16x16x32_f16 v[4:7], a[60:63], v[52:55], v[4:7]\n\t"
      "v_cvt_pk_f16_f32 v105, v2, v3 clamp\n\t"
      "v_pk_mul_f16 v63, v79, v95\n\t"
      "v_mfma_f32_16x16x32_f16 v[0:3], a[64:67], v[40:43], v[24:27]\n\t"
      "ds_read_b128 v[92:95], v212 offset:64\n\t"
      "v_pk_fmac_f16 v112, v104, v124\n\t"
      "v_mfma_f32_16x16x32_f16 v[0:3], a[68:71], v[44:47], v[0:3]\n\t"
      "s_waitcnt lgkmcnt(3)\n\t"
      "ds_read_b128 v[76:79], v213 offset:3520\n\t"
      "v_pk_fmac_f16 v113, v105, v125\n\t"
      "v_pk_mul_f16 v64, v80, v96\n\t"
      "v_mfma_f32_16x16x32_f16 v[0:3], a[72:75], v[48:51], v[0:3]\n\t"
      "v_cvt_pk_f16_f32 v104, v4, v5 clamp\n\t"
      "v_pk_maximum3_f16 v243, v243, v106, v107\n\t"
      "v_mfma_f32_16x16x32_f16 v[0:3], a[76:79], v[52:55], v[0:3]\n\t"
      "v_cvt_pk_f16_f32 v105, v6, v7 clamp\n\t"
      "v_pk_mul_f16 v65, v81, v97\n\t"
      "v_mfma_f32_16x16x32_f16 v[4:7], a[80:83], v[40:43], v[28:31]\n\t"
      "v_pk_fmac_f16 v114, v104, v126\n\t"
      "v_pk_sub_u16 v106, v106, s44 clamp\n\t"
      "v_mfma_f32_16x16x32_f16 v[4:7], a[84:87], v[44:47], v[4:7]\n\t"
      "v_pk_fmac_f16 v115, v105, v127\n\t"
      "v_pk_mul_f16 v66, v82, v98\n\t"
      "v_mfma_f32_16x16x32_f16 v[4:7], a[88:91], v[48:51], v[4:7]\n\t"
      "v_cvt_pk_f16_f32 v104, v0, v1 clamp\n\t"
      "v_and_b32 v154, s37, v154\n\t"
      "v_mfma_f32_16x16x32_f16 v[4:7], a[92:95], v[52:55], v[4:7]\n\t"
      "v_cvt_pk_f16_f32 v105, v2, v3 clamp\n\t"
      "v_pk_mul_f16 v67, v83, v99\n\t"
      "v_mfma_f32_16x16x32_f16 v[0:3], a[96:99], v[40:43], v[32:35]\n\t"
      "ds_read_b128 v[96:99], v212 offset:128\n\t"
      "v_pk_fmac_f16 v112, v104, v128\n\t"
      "v_mfma_f32_16x16x32_f16 v[0:3], a[100:103], v[44:47], v[0:3]\n\t"
      "ds_read_b128 v[80:83], v213 offset:3584\n\t"
      "v_pk_fmac_f16 v113, v105, v129\n\t"
      "v_pk_mul_f16 v68, v84, v100\n\t"
      "v_mfma_f32_16x16x32_f16 v[0:3], a[104:107], v[48:51], v[0:3]\n\t"
      "v_cvt_pk_f16_f32 v104, v4, v5 clamp\n\t"
      "v_pk_sub_u16 v107, v107, s44 clamp\n\t"
      "v_mfma_f32_16x16x32_f16 v[0:3], a[108:111], v[52:55], v[0:3]\n\t"
      "v_cvt_pk_f16_f32 v105, v6, v7 clamp\n\t"
      "v_pk_mul_f16 v69, v85, v101\n\t"
      "v_mfma_f32_16x16x32_f16 v[4:7], a[112:115], v[40:43], v[36:39]\n\t"
      "v_pk_fmac_f16 v114, v104, v130\n\t"
      "v_pk_mul_f16 v70, v86, v102\n\t"
      "v_mfma_f32_16x16x32_f16 v[4:7], a[116:119], v[44:47], v[4:7]\n\t"
      "v_pk_fmac_f16 v115, v105, v131\n\t"
      "v_pk_mul_f16 v71, v87, v103\n\t"
      "v_and_b32 v155, s37, v155\n\t"
      "v_mfma_f32_16x16x32_f16 v[4:7], a[120:123], v[48:51], v[4:7]\n\t"
      "ds_read_b128 v[100:103], v212 offset:192\n\t"
      "v_cvt_pk_f16_f32 v104, v0, v1 clamp\n\t"
      "v_mfma_f32_16x16x32_f16 v[4:7], a[124:127], v[52:55], v[4:7]\n\t"
      "ds_read_b128 v[84:87], v213 offset:3648\n\t"
      "v_cvt_pk_f16_f32 v105, v2, v3 clamp\n\t"
      "v_mfma_f32_16x16x32_f16 v[0:3], a[0:3], v[56:59], v[8:11]\n\t"
      "v_pk_fmac_f16 v112, v104, v132\n\t"
      "v_lshl_or_b32 v154, v106, 3, v154\n\t"
      "v_mfma_f32_16x16x32_f16 v[0:3], a[4:7], v[60:63], v[0:3]\n\t"
      "s_waitcnt lgkmcnt(4)\n\t"
      "v_pk_fmac_f16 v113, v105, v133\n\t"
      "v_pk_mul_f16 v40, v72, v88\n\t"
      "v_mfma_f32_16x16x32_f16 v[0:3], a[8:11], v[64:67], v[0:3]\n\t"
      "v_cvt_pk_f16_f32 v104, v4, v5 clamp\n\t"
      "v_lshl_or_b32 v155, v107, 3, v155\n\t"
      "v_mfma_f32_16x16x32_f16 v[0:3], a[12:15], v[68:71], v[0:3]\n\t"
      "v_cvt_pk_f16_f32 v105, v6, v7 clamp\n\t"
      "v_pk_mul_f16 v41, v73, v89\n\t"
      "v_mfma_f32_16x16x32_f16 v[4:7], a[16:19], v[56:59], v[12:15]\n\t"
      "ds_write_b128 v220, v[152:155] offset:4608\n\t"
      "v_pk_fmac_f16 v114, v104, v134\n\t"
      "v_mfma_f32_16x16x32_f16 v[4:7], a[20:23], v[60:63], v[4:7]\n\t"
      "v_pk_fmac_f16 v115, v105, v135\n\t"
      "v_pk_mul_f16 v42, v74, v90\n\t"
      "v_mfma_f32_16x16x32_f16 v[4:7], a[24:27], v[64:67], v[4:7]\n\t"
      "s_waitcnt vmcnt(7)\n\t"
      "v_cvt_pk_f16_f32 v104, v0, v1 clamp\n\t"
      "v_and_b32 v106, s36, v156\n\t"
      "v_mfma_f32_16x16x32_f16 v[4:7], a[28:31], v[68:71], v[4:7]\n\t"
      "v_cvt_pk_f16_f32 v105, v2, v3 clamp\n\t"
      "v_pk_mul_f16 v43, v75, v91\n\t"
      "v_mfma_f32_16x16x32_f16 v[116:119], a[136:139], v[112:115], v[116:119]\n\t"
      "ds_read_b128 v[88:91], v212 offset:1152\n\t"
      "v_and_b32 v107, s36, v157\n\t"
      "v_mfma_f32_16x16x32_f16 v[0:3], a[32:35], v[56:59], v[16:19]\n\t"
      "v_pk_mul_f16 v112, v104, v120\n\t"
      "v_pk_maximum3_f16 v243, v243, v106, v107\n\t"
      "v_mfma_f32_16x16x32_f16 v[0:3], a[36:39], v[60:63], v[0:3]\n\t"
      "v_pk_mul_f16 v113, v105, v121\n\t"
      "v_pk_mul_f16 v44, v76, v92\n\t"
      "v_mfma_f32_16x16x32_f16 v[0:3], a[40:43], v[64:67], v[0:3]\n\t"
      "v_cvt_pk_f16_f32 v104, v4, v5 clamp\n\t"
      "v_pk_sub_u16 v106, v106, s44 clamp\n\t"
      "v_mfma_f32_16x16x32_f16 v[0:3], a[44:47], v[68:71], v[0:3]\n\t"
      "v_cvt_pk_f16_f32 v105, v6, v7 clamp\n\t"
      "v_pk_mul_f16 v45, v77, v93\n\t"
      "v_mfma_f32_16x16x32_f16 v[4:7], a[48:51], v[56:59], v[20:23]\n\t"
      "v_pk_mul_f16 v114, v104, v122\n\t"
      "v_and_b32 v156, s37, v156\n\t"
      "v_mfma_f32_16x16x32_f16 v[4:7], a[52:55], v[60:63], v[4:7]\n\t"
      "v_pk_mul_f16 v115, v105, v123\n\t"
      "v_pk_mul_f16 v46, v78, v94\n\t"
      "v_mfma_f32_16x16x32_f16 v[4:7], a[56:59], v[64:67], v[4:7]\n\t"
      "v_cvt_pk_f16_f32 v104, v0, v1 clamp\n\t"
      "v_pk_sub_u16 v107, v107, s44 clamp\n\t"
      "v_mfma_f32_16x16x32_f16 v[4:7], a[60:63], v[68:71], v[4:7]\n\t"
      "v_cvt_pk_f16_f32 v105, v2, v3 clamp\n\t"
      "v_pk_mul_f16 v47, v79, v95\n\t"
      "v_mfma_f32_16x16x32_f16 v[0:3], a[64:67], v[56:59], v[24:27]\n\t"
      "ds_read_b128 v[92:95], v212 offset:1216\n\t"
      "v_pk_fmac_f16 v112, v104, v124\n\t"
      "v_mfma_f32_16x16x32_f16 v[0:3], a[68:71], v[60:63], v[0:3]\n\t"
      "s_waitcnt lgkmcnt(3)\n\t"
      "v_pk_fmac_f16 v113, v105, v125\n\t"
      "v_pk_mul_f16 v48, v80, v96\n\t"
      "v_mfma_f32_16x16x32_f16 v[0:3], a[72:75], v[64:67], v[0:3]\n\t"
      "v_cvt_pk_f16_f32 v104, v4, v5 clamp\n\t"
      "v_and_b32 v157, s37, v157\n\t"
      "v_mfma_f32_16x16x32_f16 v[0:3], a[76:79], v[68:71], v[0:3]\n\t"
      "v_cvt_pk_f16_f32 v105, v6, v7 clamp\n\t"
      "v_pk_mul_f16 v49, v81, v97\n\t"
      "v_mfma_f32_16x16x32_f16 v[4:7], a[80:83], v[56:59], v[28:31]\n\t"
      "v_pk_fmac_f16 v114, v104, v126\n\t"
      "v_lshl_or_b32 v156, v106, 3, v156\n\t"
      "v_mfma_f32_16x16x32_f16 v[4:7], a[84:87], v[60:63], v[4:7]\n\t"
      "v_pk_fmac_f16 v115, v105, v127\n\t"
      "v_pk_mul_f16 v50, v82, v98\n\t"
      "v_mfma_f32_16x16x32_f16 v[4:7], a[88:91], v[64:67], v[4:7]\n\t"
      "v_cvt_pk_f16_f32 v104, v0, v1 clamp\n\t"
      "v_lshl_or_b32 v157, v107, 3, v157\n\t"
      "v_mfma_f32_16x16x32_f16 v[4:7], a[92:95], v[68:71], v[4:7]\n\t"
      "v_cvt_pk_f16_f32 v105, v2, v3 clamp\n\t"
      "v_pk_mul_f16 v51, v83, v99\n\t"
      "v_mfma_f32_16x16x32_f16 v[0:3], a[96:99], v[56:59], v[32:35]\n\t"
      "ds_read_b128 v[96:99], v212 offset:1280\n\t"
      "v_pk_fmac_f16 v112, v104, v128\n\t"
      "v_mfma_f32_16x16x32_f16 v[0:3], a[100:103], v[60:63], v[0:3]\n\t"
      "v_pk_fmac_f16 v113, v105, v129\n\t"
      "v_pk_mul_f16 v52, v84, v100\n\t"
      "v_mfma_f32_16x16x32_f16 v[0:3], a[104:107], v[64:67], v[0:3]\n\t"
      "v_cvt_pk_f16_f32 v104, v4, v5 clamp\n\t"
      "v_and_b32 v106, s36, v158\n\t"
      "v_mfma_f32_16x16x32_f16 v[0:3], a[108:111], v[68:71], v[0:3]\n\t"
      "v_cvt_pk_f16_f32 v105, v6, v7 clamp\n\t"
      "v_pk_mul_f16 v53, v85, v101\n\t"
      "v_mfma_f32_16x16x32_f16 v[4:7], a[112:115], v[56:59], v[36:39]\n\t"
      "v_pk_fmac_f16 v114, v104, v130\n\t"
      "v_pk_mul_f16 v54, v86, v102\n\t"
      "v_mfma_f32_16x16x32_f16 v[4:7], a[116:119], v[60:63], v[4:7]\n\t"
      "v_pk_fmac_f16 v115, v105, v131\n\t"
      "v_pk_mul_f16 v55, v87, v103\n\t"
      "v_mfma_f32_16x16x32_f16 v[4:7], a[120:123], v[64:67], v[4:7]\n\t"
      "ds_read_b128 v[100:103], v212 offset:1344\n\t"
      "v_cvt_pk_f16_f32 v104, v0, v1 clamp\n\t"
      "v_mfma_f32_16x16x32_f16 v[4:7], a[124:127], v[68:71], v[4:7]\n\t"
      "v_cvt_pk_f16_f32 v105, v2, v3 clamp\n\t"
      "v_and_b32 v107, s36, v159\n\t"
      "v_mfma_f32_16x16x32_f16 v[0:3], a[0:3], v[40:43], v[8:11]\n\t"
      "v_pk_fmac_f16 v112, v104, v132\n\t"
      "v_pk_maximum3_f16 v243, v243, v106, v107\n\t"
      "v_mfma_f32_16x16x32_f16 v[0:3], a[4:7], v[44:47], v[0:3]\n\t"
      "s_waitcnt lgkmcnt(2)\n\t"
      "v_pk_fmac_f16 v113, v105, v133\n\t"
      "v_pk_mul_f16 v56, v72, v88\n\t"
      "v_mfma_f32_16x16x32_f16 v[0:3], a[8:11], v[48:51], v[0:3]\n\t"
      "v_cvt_pk_f16_f32 v104, v4, v5 clamp\n\t"
      "v_pk_sub_u16 v106, v106, s44 clamp\n\t"
      "v_mfma_f32_16x16x32_f16 v[0:3], a[12:15], v[52:55], v[0:3]\n\t"
      "v_cvt_pk_f16_f32 v105, v6, v7 clamp\n\t"
      "v_pk_mul_f16 v57, v73, v89\n\t"
      "v_mfma_f32_16x16x32_f16 v[4:7], a[16:19], v[40:43], v[12:15]\n\t"
      "v_pk_fmac_f16 v114, v104, v134\n\t"
      "v_and_b32 v158, s37, v158\n\t"
      "v_mfma_f32_16x16x32_f16 v[4:7], a[20:23], v[44:47], v[4:7]\n\t"
      "v_pk_fmac_f16 v115, v105, v135\n\t"
      "v_pk_mul_f16 v58, v74, v90\n\t"
      "v_mfma_f32_16x16x32_f16 v[4:7], a[24:27], v[48:51], v[4:7]\n\t"
      "v_cvt_pk_f16_f32 v104, v0, v1 clamp\n\t"
      "v_pk_sub_u16 v107, v107, s44 clamp\n\t"
      "v_mfma_f32_16x16x32_f16 v[4:7], a[28:31], v[52:55], v[4:7]\n\t"
      "v_cvt_pk_f16_f32 v105, v2, v3 clamp\n\t"
      "v_pk_mul_f16 v59, v75, v91\n\t"
      "v_mfma_f32_16x16x32_f16 v[116:119], a[140:143], v[112:115], v[116:119]\n\t"
      "ds_read_b128 v[88:91], v212 offset:2304\n\t"
      "v_and_b32 v159, s37, v159\n\t"
      "v_mfma_f32_16x16x32_f16 v[0:3], a[32:35], v[40:43], v[16:19]\n\t"
      "v_pk_mul_f16 v112, v104, v120\n\t"
      "v_lshl_or_b32 v158, v106, 3, v158\n\t"
      "v_mfma_f32_16x16x32_f16 v[0:3], a[36:39], v[44:47], v[0:3]\n\t"
      "v_pk_mul_f16 v113, v105, v121\n\t"
      "v_pk_mul_f16 v60, v76, v92\n\t"
      "v_mfma_f32_16x16x32_f16 v[0:3], a[40:43], v[48:51], v[0:3]\n\t"
      "v_cvt_pk_f16_f32 v104, v4, v5 clamp\n\t"
      "v_lshl_or_b32 v159, v107, 3, v159\n\t"
      "v_mfma_f32_16x16x32_f16 v[0:3], a[44:47], v[52:55], v[0:3]\n\t"
      "ds_write_b128 v220, v[156:159] offset:5760\n\t"
      "v_cvt_pk_f16_f32 v105, v6, v7 clamp\n\t"
      "v_pk_mul_f16 v61, v77, v93\n\t"
      "v_mfma_f32_16x16x32_f16 v[4:7], a[48:51], v[40:43], v[20:23]\n\t"
      "ds_write_b32 v218, v116 offset:0\n\t"
      "v_pk_mul_f16 v114, v104, v122\n\t"
      "v_mfma_f32_16x16x32_f16 v[4:7], a[52:55], v[44:47], v[4:7]\n\t"
      "v_pk_mul_f16 v115, v105, v123\n\t"
      "v_pk_mul_f16 v62, v78, v94\n\t"
      "v_mfma_f32_16x16x32_f16 v[4:7], a[56:59], v[48:51], v[4:7]\n\t"
      "ds_write_b128 v222, v[156:159]\n\t"
      "v_cvt_pk_f16_f32 v104, v0, v1 clamp\n\t"
      "v_mfma_f32_16x16x32_f16 v[4:7], a[60:63], v[52:55], v[4:7]\n\t"
      "v_cvt_pk_f16_f32 v105, v2, v3 clamp\n\t"
      "v_pk_mul_f16 v63, v79, v95\n\t"
      "v_mfma_f32_16x16x32_f16 v[0:3], a[64:67], v[40:43], v[24:27]\n\t"
      "ds_read_b128 v[92:95], v212 offset:2368\n\t"
      "v_pk_fmac_f16 v112, v104, v124\n\t"
      "v_mfma_f32_16x16x32_f16 v[0:3], a[68:71], v[44:47], v[0:3]\n\t"
      "s_waitcnt lgkmcnt(5)\n\t"
      "v_pk_fmac_f16 v113, v105, v125\n\t"
      "v_pk_mul_f16 v64, v80, v96\n\t"
      "v_mfma_f32_16x16x32_f16 v[0:3], a[72:75], v[48:51], v[0:3]\n\t"
      "s_waitcnt vmcnt(6)\n\t"
      "v_cvt_pk_f16_f32 v104, v4, v5 clamp\n\t"
      "v_and_b32 v106, s36, v160\n\t"
      "v_mfma_f32_16x16x32_f16 v[0:3], a[76:79], v[52:55], v[0:3]\n\t"
      "v_cvt_pk_f16_f32 v105, v6, v7 clamp\n\t"
      "v_pk_mul_f16 v65, v81, v97\n\t"
      "v_mfma_f32_16x16x32_f16 v[4:7], a[80:83], v[40:43], v[28:31]\n\t"
      "v_pk_fmac_f16 v114, v104, v126\n\t"
      "v_and_b32 v107, s36, v161\n\t"
      "v_mfma_f32_16x16x32_f16 v[4:7], a[84:87], v[44:47], v[4:7]\n\t"
      "v_pk_fmac_f16 v115, v105, v127\n\t"
      "v_pk_mul_f16 v66, v82, v98\n\t"
      "v_mfma_f32_16x16x32_f16 v[4:7], a[88:91], v[48:51], v[4:7]\n\t"
      "v_cvt_pk_f16_f32 v104, v0, v1 clamp\n\t"
      "v_pk_maximum3_f16 v243, v243, v106, v107\n\t"
      "v_mfma_f32_16x16x32_f16 v[4:7], a[92:95], v[52:55], v[4:7]\n\t"
      "v_cvt_pk_f16_f32 v105, v2, v3 clamp\n\t"
      "v_pk_mul_f16 v67, v83, v99\n\t"
      "v_mfma_f32_16x16x32_f16 v[0:3], a[96:99], v[40:43], v[32:35]\n\t"
      "ds_read_b128 v[96:99], v212 offset:2432\n\t"
      "v_pk_fmac_f16 v112, v104, v128\n\t"
      "v_mfma_f32_16x16x32_f16 v[0:3], a[100:103], v[44:47], v[0:3]\n\t"
      "v_pk_fmac_f16 v113, v105, v129\n\t"
      "v_pk_mul_f16 v68, v84, v100\n\t"
      "v_mfma_f32_16x16x32_f16 v[0:3], a[104:107], v[48:51], v[0:3]\n\t"
      "v_cvt_pk_f16_f32 v104, v4, v5 clamp\n\t"
      "v_pk_sub_u16 v106, v106, s44 clamp\n\t"
      "v_mfma_f32_16x16x32_f16 v[0:3], a[108:111], v[52:55], v[0:3]\n\t"
      "v_cvt_pk_f16_f32 v105, v6, v7 clamp\n\t"
      "v_pk_mul_f16 v69, v85, v101\n\t"
      "v_mfma_f32_16x16x32_f16 v[4:7], a[112:115], v[40:43], v[36:39]\n\t"
      "v_pk_fmac_f16 v114, v104, v130\n\t"
      "v_pk_mul_f16 v70, v86, v102\n\t"
      "v_mfma_f32_16x16x32_f16 v[4:7], a[116:119], v[44:47], v[4:7]\n\t"
      "v_pk_fmac_f16 v115, v105, v131\n\t"
      "v_pk_mul_f16 v71, v87, v103\n\t"
      "v_mfma_f32_16x16x32_f16 v[4:7], a[120:123], v[48:51], v[4:7]\n\t"
      "ds_read_b128 v[100:103], v212 offset:2496\n\t"
      "v_cvt_pk_f16_f32 v104, v0, v1 clamp\n\t"
      "v_mfma_f32_16x16x32_f16 v[4:7], a[124:127], v[52:55], v[4:7]\n\t"
      "v_cvt_pk_f16_f32 v105, v2, v3 clamp\n\t"
      "v_and_b32 v160, s37, v160\n\t"
      "v_mfma_f32_16x16x32_f16 v[0:3], a[0:3], v[56:59], v[8:11]\n\t"
      "v_pk_fmac_f16 v112, v104, v132\n\t"
      "v_pk_sub_u16 v107, v107, s44 clamp\n\t"
      "v_mfma_f32_16x16x32_f16 v[0:3], a[4:7], v[60:63], v[0:3]\n\t"
      "s_waitcnt lgkmcnt(2)\n\t"
      "v_pk_fmac_f16 v113, v105, v133\n\t"
      "v_pk_mul_f16 v40, v72, v88\n\t"
      "v_mfma_f32_16x16x32_f16 v[0:3], a[8:11], v[64:67], v[0:3]\n\t"
      "v_cvt_pk_f16_f32 v104, v4, v5 clamp\n\t"
      "v_and_b32 v161, s37, v161\n\t"
      "v_mfma_f32_16x16x32_f16 v[0:3], a[12:15], v[68:71], v[0:3]\n\t"
      "v_cvt_pk_f16_f32 v105, v6, v7 clamp\n\t"
      "v_pk_mul_f16 v41, v73, v89\n\t"
      "v_mfma_f32_16x16x32_f16 v[4:7], a[16:19], v[56:59], v[12:15]\n\t"
      "v_pk_fmac_f16 v114, v104, v134\n\t"
      "v_lshl_or_b32 v160, v106, 3, v160\n\t"
      "v_mfma_f32_16x16x32_f16 v[4:7], a[20:23], v[60:63], v[4:7]\n\t"
      "v_pk_fmac_f16 v115, v105, v135\n\t"
      "v_pk_mul_f16 v42, v74, v90\n\t"
      "v_mfma_f32_16x16x32_f16 v[4:7], a[24:27], v[64:67], v[4:7]\n\t"
      "v_cvt_pk_f16_f32 v104, v0, v1 clamp\n\t"
      "v_lshl_or_b32 v161, v107, 3, v161\n\t"
      "v_mfma_f32_16x16x32_f16 v[4:7], a[28:31], v[68:71], v[4:7]\n\t"
      "v_cvt_pk_f16_f32 v105, v2, v3 clamp\n\t"
      "v_pk_mul_f16 v43, v75, v91\n\t"
      "v_mfma_f32_16x16x32_f16 v[116:119], a[128:131], v[112:115], 0\n\t"
      "ds_read_b128 v[88:91], v212 offset:0\n\t"
      "v_and_b32 v106, s36, v162\n\t"
      "v_mfma_f32_16x16x32_f16 v[0:3], a[32:35], v[56:59], v[16:19]\n\t"
      "ds_read_b128 v[72:75], v213 offset:4608\n\t"
      "v_pk_mul_f16 v112, v104, v120\n\t"
      "v_mfma_f32_16x16x32_f16 v[0:3], a[36:39], v[60:63], v[0:3]\n\t"
      "v_pk_mul_f16 v113, v105, v121\n\t"
      "v_pk_mul_f16 v44, v76, v92\n\t"
      "v_mfma_f32_16x16x32_f16 v[0:3], a[40:43], v[64:67], v[0:3]\n\t"
      "v_cvt_pk_f16_f32 v104, v4, v5 clamp\n\t"
      "v_and_b32 v107, s36, v163\n\t"
      "v_mfma_f32_16x16x32_f16 v[0:3], a[44:47], v[68:71], v[0:3]\n\t"
      "v_cvt_pk_f16_f32 v105, v6, v7 clamp\n\t"
      "v_pk_mul_f16 v45, v77, v93\n\t"
      "v_mfma_f32_16x16x32_f16 v[4:7], a[48:51], v[56:59], v[20:23]\n\t"
      "v_pk_mul_f16 v114, v104, v122\n\t"
      "v_pk_maximum3_f16 v243, v243, v106, v107\n\t"
      "v_mfma_f32_16x16x32_f16 v[4:7], a[52:55], v[60:63], v[4:7]\n\t"
      "v_pk_mul_f16 v115, v105, v123\n\t"
      "v_pk_mul_f16 v46, v78, v94\n\t"
      "v_mfma_f32_16x16x32_f16 v[4:7], a[56:59], v[64:67], v[4:7]\n\t"
      "v_cvt_pk_f16_f32 v104, v0, v1 clamp\n\t"
      "v_pk_sub_u16 v106, v106, s44 clamp\n\t"
      "v_mfma_f32_16x16x32_f16 v[4:7], a[60:63], v[68:71], v[4:7]\n\t"
      "v_cvt_pk_f16_f32 v105, v2, v3 clamp\n\t"
      "v_pk_mul_f16 v47, v79, v95\n\t"
      "v_mfma_f32_16x16x32_f16 v[0:3], a[64:67], v[56:59], v[24:27]\n\t"
      "ds_read_b128 v[92:95], v212 offset:64\n\t"
      "v_pk_fmac_f16 v112, v104, v124\n\t"
      "v_mfma_f32_16x16x32_f16 v[0:3], a[68:71], v[60:63], v[0:3]\n\t"
      "s_waitcnt lgkmcnt(3)\n\t"
      "ds_read_b128 v[76:79], v213 offset:4672\n\t"
      "v_pk_fmac_f16 v113, v105, v125\n\t"
      "v_pk_mul_f16 v48, v80, v96\n\t"
      "v_mfma_f32_16x16x32_f16 v[0:3], a[72:75], v[64:67], v[0:3]\n\t"
      "v_cvt_pk_f16_f32 v104, v4, v5 clamp\n\t"
      "v_and_b32 v162, s37, v162\n\t"
      "v_mfma_f32_16x16x32_f16 v[0:3], a[76:79], v[68:71], v[0:3]\n\t"
      "v_cvt_pk_f16_f32 v105, v6, v7 clamp\n\t"
      "v_pk_mul_f16 v49, v81, v97\n\t"
      "v_mfma_f32_16x16x32_f16 v[4:7], a[80:83], v[56:59], v[28:31]\n\t"
      "v_pk_fmac_f16 v114, v104, v126\n\t"
      "v_pk_sub_u16 v107, v107, s44 clamp\n\t"
      "v_mfma_f32_16x16x32_f16 v[4:7], a[84:87], v[60:63], v[4:7]\n\t"
      "v_pk_fmac_f16 v115, v105, v127\n\t"
      "v_pk_mul_f16 v50, v82, v98\n\t"
      "v_mfma_f32_16x16x32_f16 v[4:7], a[88:91], v[64:67], v[4:7]\n\t"
      "v_cvt_pk_f16_f32 v104, v0, v1 clamp\n\t"
      "v_and_b32 v163, s37, v163\n\t"
      "v_mfma_f32_16x16x32_f16 v[4:7], a[92:95], v[68:71], v[4:7]\n\t"
      "v_cvt_pk_f16_f32 v105, v2, v3 clamp\n\t"
      "v_pk_mul_f16 v51, v83, v99\n\t"
      "v_mfma_f32_16x16x32_f16 v[0:3], a[96:99], v[56:59], v[32:35]\n\t"
      "ds_read_b128 v[96:99], v212 offset:128\n\t"
      "v_pk_fmac_f16 v112, v104, v128\n\t"
      "v_mfma_f32_16x16x32_f16 v[0:3], a[100:103], v[60:63], v[0:3]\n\t"
      "ds_read_b128 v[80:83], v213 offset:4736\n\t"
      "v_pk_fmac_f16 v113, v105, v129\n\t"
      "v_pk_mul_f16 v52, v84, v100\n\t"
      "v_mfma_f32_16x16x32_f16 v[0:3], a[104:107], v[64:67], v[0:3]\n\t"
      "v_cvt_pk_f16_f32 v104, v4, v5 clamp\n\t"
      "v_lshl_or_b32 v162, v106, 3, v162\n\t"
      "v_lshl_or_b32 v163, v107, 3, v163\n\t"
      "v_mfma_f32_16x16x32_f16 v[0:3], a[108:111], v[68:71], v[0:3]\n\t"
      "v_cvt_pk_f16_f32 v105, v6, v7 clamp\n\t"
      "v_pk_mul_f16 v53, v85, v101\n\t"
      "v_mfma_f32_16x16x32_f16 v[4:7], a[112:115], v[56:59], v[36:39]\n\t"
      "v_pk_fmac_f16 v114, v104, v130\n\t"
      "v_pk_mul_f16 v54, v86, v102\n\t"
      "v_mfma_f32_16x16x32_f16 v[4:7], a[116:119], v[60:63], v[4:7]\n\t"
      "v_pk_fmac_f16 v115, v105, v131\n\t"
      "v_pk_mul_f16 v55, v87, v103\n\t"
      "v_mfma_f32_16x16x32_f16 v[4:7], a[120:123], v[64:67], v[4:7]\n\t"
      "ds_read_b128 v[100:103], v212 offset:192\n\t"
      "v_cvt_pk_f16_f32 v104, v0, v1 clamp\n\t"
      "v_mfma_f32_16x16x32_f16 v[4:7], a[124:127], v[68:71], v[4:7]\n\t"
      "ds_read_b128 v[84:87], v213 offset:4800\n\t"
      "v_cvt_pk_f16_f32 v105, v2, v3 clamp\n\t"
      "v_mfma_f32_16x16x32_f16 v[0:3], a[0:3], v[40:43], v[8:11]\n\t"
      "ds_write_b128 v221, v[160:163]\n\t"
      "v_pk_fmac_f16 v112, v104, v132\n\t"
      "v_mfma_f32_16x16x32_f16 v[0:3], a[4:7], v[44:47], v[0:3]\n\t"
      "s_waitcnt lgkmcnt(5)\n\t"
      "v_pk_fmac_f16 v113, v105, v133\n\t"
      "v_pk_mul_f16 v56, v72, v88\n\t"
      "v_mfma_f32_16x16x32_f16 v[0:3], a[8:11], v[48:51], v[0:3]\n\t"
      "ds_write_b128 v223, v[160:163]\n\t"
      "v_cvt_pk_f16_f32 v104, v4, v5 clamp\n\t"
      "v_mfma_f32_16x16x32_f16 v[0:3], a[12:15], v[52:55], v[0:3]\n\t"
      "v_cvt_pk_f16_f32 v105, v6, v7 clamp\n\t"
      "v_pk_mul_f16 v57, v73, v89\n\t"
      "v_mfma_f32_16x16x32_f16 v[4:7], a[16:19], v[40:43], v[12:15]\n\t"
      "s_waitcnt vmcnt(4)\n\t"
      "v_pk_fmac_f16 v114, v104, v134\n\t"
      "v_lshl_add_u32 v148, v252, 8, v228\n\t"
      "v_mfma_f32_16x16x32_f16 v[4:7], a[20:23], v[44:47], v[4:7]\n\t"
      "global_load_dwordx4 v[148:151], v148, s[40:41]\n\t"
      "v_pk_fmac_f16 v115, v105, v135\n\t"
      "v_pk_mul_f16 v58, v74, v90\n\t"
      "v_mfma_f32_16x16x32_f16 v[4:7], a[24:27], v[48:51], v[4:7]\n\t"
      "v_cvt_pk_f16_f32 v104, v0, v1 clamp\n\t"
      "v_lshl_add_u32 v152, v253, 8, v228\n\t"
      "v_mfma_f32_16x16x32_f16 v[4:7], a[28:31], v[52:55], v[4:7]\n\t"
      "v_cvt_pk_f16_f32 v105, v2, v3 clamp\n\t"
      "v_pk_mul_f16 v59, v75, v91\n\t"
      "v_mfma_f32_16x16x32_f16 v[116:119], a[132:135], v[112:115], v[116:119]\n\t"
      "ds_read_b128 v[88:91], v212 offset:1152\n\t"
      "global_load_dwordx4 v[152:155], v152, s[40:41]\n\t"
      "v_lshl_add_u32 v156, v254, 8, v228\n\t"
      "v_mfma_f32_16x16x32_f16 v[0:3], a[32:35], v[40:43], v[16:19]\n\t"
      "v_pk_mul_f16 v112, v104, v120\n\t"
      "v_lshl_add_u32 v160, v251, 8, v228\n\t"
      "v_mfma_f32_16x16x32_f16 v[0:3], a[36:39], v[44:47], v[0:3]\n\t"
      "v_pk_mul_f16 v113, v105, v121\n\t"
      "v_pk_mul_f16 v60, v76, v92\n\t"
      "v_mfma_f32_16x16x32_f16 v[0:3], a[40:43], v[48:51], v[0:3]\n\t"
      "global_load_dwordx4 v[156:159], v156, s[40:41]\n\t"
      "ds_read_b32 v202, v219 offset:0\n\t"
      "v_cvt_pk_f16_f32 v104, v4, v5 clamp\n\t"
      "v_mfma_f32_16x16x32_f16 v[0:3], a[44:47], v[52:55], v[0:3]\n\t"
      "v_cvt_pk_f16_f32 v105, v6, v7 clamp\n\t"
      "v_pk_mul_f16 v61, v77, v93\n\t"
      "v_mfma_f32_16x16x32_f16 v[4:7], a[48:51], v[40:43], v[20:23]\n\t"
      "ds_read_b32 v203, v219 offset:256\n\t"
      "v_pk_mul_f16 v114, v104, v122\n\t"
      "v_mfma_f32_16x16x32_f16 v[4:7], a[52:55], v[44:47], v[4:7]\n\t"
      "global_load_dwordx4 v[160:163], v160, s[40:41]\n\t"
      "v_pk_mul_f16 v115, v105, v123\n\t"
      "v_pk_mul_f16 v62, v78, v94\n\t"
      "v_mfma_f32_16x16x32_f16 v[4:7], a[56:59], v[48:51], v[4:7]\n\t"
      "ds_read_b32 v204, v219 offset:512\n\t"
      "v_cvt_pk_f16_f32 v104, v0, v1 clamp\n\t"
      "v_mfma_f32_16x16x32_f16 v[4:7], a[60:63], v[52:55], v[4:7]\n\t"
      "v_cvt_pk_f16_f32 v105, v2, v3 clamp\n\t"
      "v_pk_mul_f16 v63, v79, v95\n\t"
      "v_mfma_f32_16x16x32_f16 v[0:3], a[64:67], v[40:43], v[24:27]\n\t"
      "ds_read_b128 v[92:95], v212 offset:1216\n\t"
      "global_load_dwordx3 v[252:254], v226, s[42:43] offset:12\n\t"
      "v_pk_fmac_f16 v112, v104, v124\n\t"
      "v_mfma_f32_16x16x32_f16 v[0:3], a[68:71], v[44:47], v[0:3]\n\t"
      "s_waitcnt lgkmcnt(7)\n\t"
      "v_pk_fmac_f16 v113, v105, v125\n\t"
      "v_pk_mul_f16 v64, v80, v96\n\t"
      "v_mfma_f32_16x16x32_f16 v[0:3], a[72:75], v[48:51], v[0:3]\n\t"
      "ds_read_b32 v205, v219 offset:768\n\t"
      "v_cvt_pk_f16_f32 v104, v4, v5 clamp\n\t"
      "v_mfma_f32_16x16x32_f16 v[0:3], a[76:79], v[52:55], v[0:3]\n\t"
      "global_load_dword v251, v227, s[42:43]\n\t"
      "v_cvt_pk_f16_f32 v105, v6, v7 clamp\n\t"
      "v_pk_mul_f16 v65, v81, v97\n\t"
      "v_mfma_f32_16x16x32_f16 v[4:7], a[80:83], v[40:43], v[28:31]\n\t"
      "ds_read_b32 v206, v219 offset:1024\n\t"
      "v_pk_fmac_f16 v114, v104, v126\n\t"
      "v_mfma_f32_16x16x32_f16 v[4:7], a[84:87], v[44:47], v[4:7]\n\t"
      "v_pk_fmac_f16 v115, v105, v127\n\t"
      "v_pk_mul_f16 v66, v82, v98\n\t"
      "v_mfma_f32_16x16x32_f16 v[4:7], a[88:91], v[48:51], v[4:7]\n\t"
      "v_cvt_pk_f16_f32 v104, v0, v1 clamp\n\t"
      "v_add_u32 v219, s46, v219\n\t"
      "v_mfma_f32_16x16x32_f16 v[4:7], a[92:95], v[52:55], v[4:7]\n\t"
      "v_cvt_pk_f16_f32 v105, v2, v3 clamp\n\t"
      "v_pk_mul_f16 v67, v83, v99\n\t"
      "v_mfma_f32_16x16x32_f16 v[0:3], a[96:99], v[40:43], v[32:35]\n\t"
      "ds_read_b128 v[96:99], v212 offset:1280\n\t"
      "v_pk_fmac_f16 v112, v104, v128\n\t"
      "v_mfma_f32_16x16x32_f16 v[0:3], a[100:103], v[44:47], v[0:3]\n\t"
      "v_pk_fmac_f16 v113, v105, v129\n\t"
      "v_pk_mul_f16 v68, v84, v100\n\t"
      "v_mfma_f32_16x16x32_f16 v[0:3], a[104:107], v[48:51], v[0:3]\n\t"
      "s_waitcnt lgkmcnt(1)\n\t"
      "v_cvt_pk_f16_f32 v104, v4, v5 clamp\n\t"
      "v_max3_f32 v207, v202, v203, v204\n\t"
      "v_mfma_f32_16x16x32_f16 v[0:3], a[108:111], v[52:55], v[0:3]\n\t"
      "v_cvt_pk_f16_f32 v105, v6, v7 clamp\n\t"
      "v_pk_mul_f16 v69, v85, v101\n\t"
      "v_mfma_f32_16x16x32_f16 v[4:7], a[112:115], v[40:43], v[36:39]\n\t"
      "v_pk_fmac_f16 v114, v104, v130\n\t"
      "v_pk_mul_f16 v70, v86, v102\n\t"
      "v_mfma_f32_16x16x32_f16 v[4:7], a[116:119], v[44:47], v[4:7]\n\t"
      "v_pk_fmac_f16 v115, v105, v131\n\t"
      "v_pk_mul_f16 v71, v87, v103\n\t"
      "v_mfma_f32_16x16x32_f16 v[4:7], a[120:123], v[48:51], v[4:7]\n\t"
      "ds_read_b128 v[100:103], v212 offset:1344\n\t"
      "v_cvt_pk_f16_f32 v104, v0, v1 clamp\n\t"
      "v_mfma_f32_16x16x32_f16 v[4:7], a[124:127], v[52:55], v[4:7]\n\t"
      "v_cvt_pk_f16_f32 v105, v2, v3 clamp\n\t"
      "v_max3_f32 v207, v207, v205, v206\n\t"
      "v_mfma_f32_16x16x32_f16 v[0:3], a[0:3], v[56:59], v[8:11]\n\t"
      "v_pk_fmac_f16 v112, v104, v132\n\t"
      "v_max_f32_dpp v207, v207, v207 row_shr:1 row_mask:0xf bank_mask:0xf\n\t"
      "v_mfma_f32_16x16x32_f16 v[0:3], a[4:7], v[60:63], v[0:3]\n\t"
      "v_pk_fmac_f16 v113, v105, v133\n\t"
      "v_pk_mul_f16 v40, v72, v88\n\t"
      "v_mfma_f32_16x16x32_f16 v[0:3], a[8:11], v[64:67], v[0:3]\n\t"
      "v_cvt_pk_f16_f32 v104, v4, v5 clamp\n\t"
      "v_max_f32_dpp v207, v207, v207 row_shr:2 row_mask:0xf bank_mask:0xf\n\t"
      "v_mfma_f32_16x16x32_f16 v[0:3], a[12:15], v[68:71], v[0:3]\n\t"
      "v_cvt_pk_f16_f32 v105, v6, v7 clamp\n\t"
      "v_pk_mul_f16 v41, v73, v89\n\t"
      "v_mfma_f32_16x16x32_f16 v[4:7], a[16:19], v[56:59], v[12:15]\n\t"
      "v_pk_fmac_f16 v114, v104, v134\n\t"
      "v_max_f32_dpp v207, v207, v207 row_shr:4 row_mask:0xf bank_mask:0xf\n\t"
      "v_mfma_f32_16x16x32_f16 v[4:7], a[20:23], v[60:63], v[4:7]\n\t"
      "v_pk_fmac_f16 v115, v105, v135\n\t"
      "v_pk_mul_f16 v42, v74, v90\n\t"
      "v_mfma_f32_16x16x32_f16 v[4:7], a[24:27], v[64:67], v[4:7]\n\t"
      "v_cvt_pk_f16_f32 v104, v0, v1 clamp\n\t"
      "v_max_f32_dpp v207, v207, v207 row_shr:8 row_mask:0xf bank_mask:0xf\n\t"
      "v_mfma_f32_16x16x32_f16 v[4:7], a[28:31], v[68:71], v[4:7]\n\t"
      "v_cvt_pk_f16_f32 v105, v2, v3 clamp\n\t"
      "v_pk_mul_f16 v43, v75, v91\n\t"
      "v_mfma_f32_16x16x32_f16 v[116:119], a[136:139], v[112:115], v[116:119]\n\t"
      "ds_read_b128 v[88:91], v212 offset:2304\n\t"
      "v_max_f32_dpp v207, v207, v207 row_bcast:15 row_mask:0xa bank_mask:0xf\n\t"
      "v_mfma_f32_16x16x32_f16 v[0:3], a[32:35], v[56:59], v[16:19]\n\t"
      "v_pk_mul_f16 v112, v104, v120\n\t"
      "v_max_f32_dpp v207, v207, v207 row_bcast:31 row_mask:0xc bank_mask:0xf\n\t"
      "v_mfma_f32_16x16x32_f16 v[0:3], a[36:39], v[60:63], v[0:3]\n\t"
      "v_pk_mul_f16 v113, v105, v121\n\t"
      "v_pk_mul_f16 v44, v76, v92\n\t"
      "v_mfma_f32_16x16x32_f16 v[0:3], a[40:43], v[64:67], v[0:3]\n\t"
      "v_cvt_pk_f16_f32 v104, v4, v5 clamp\n\t"
      "v_readlane_b32 s54, v207, 63\n\t"
      "v_mfma_f32_16x16x32_f16 v[0:3], a[44:47], v[68:71], v[0:3]\n\t"
      "v_cvt_pk_f16_f32 v105, v6, v7 clamp\n\t"
      "v_pk_mul_f16 v45, v77, v93\n\t"
      "v_mfma_f32_16x16x32_f16 v[4:7], a[48:51], v[56:59], v[20:23]\n\t"
      "v_pk_mul_f16 v114, v104, v122\n\t"
      "v_mul_f32 v210, s54, v108\n\t"
      "v_mfma_f32_16x16x32_f16 v[4:7], a[52:55], v[60:63], v[4:7]\n\t"
      "v_pk_mul_f16 v115, v105, v123\n\t"
      "v_pk_mul_f16 v46, v78, v94\n\t"
      "v_mfma_f32_16x16x32_f16 v[4:7], a[56:59], v[64:67], v[4:7]\n\t"
      "v_cvt_pk_f16_f32 v104, v0, v1 clamp\n\t"
      "v_writelane_b32 v244, s54, m0\n\t"
      "v_mfma_f32_16x16x32_f16 v[4:7], a[60:63], v[68:71], v[4:7]\n\t"
      "v_cvt_pk_f16_f32 v105, v2, v3 clamp\n\t"
      "v_pk_mul_f16 v47, v79, v95\n\t"
      "v_mfma_f32_16x16x32_f16 v[0:3], a[64:67], v[56:59], v[24:27]\n\t"
      "ds_read_b128 v[92:95], v212 offset:2368\n\t"
      "v_pk_fmac_f16 v112, v104, v124\n\t"
      "v_mfma_f32_16x16x32_f16 v[0:3], a[68:71], v[60:63], v[0:3]\n\t"
      "s_waitcnt lgkmcnt(2)\n\t"
      "v_pk_fmac_f16 v113, v105, v125\n\t"
      "v_pk_mul_f16 v48, v80, v96\n\t"
      "v_mfma_f32_16x16x32_f16 v[0:3], a[72:75], v[64:67], v[0:3]\n\t"
      "v_cvt_pk_f16_f32 v104, v4, v5 clamp\n\t"
      "v_fma_f32 v202, v202, v108, -v210\n\t"
      "v_mfma_f32_16x16x32_f16 v[0:3], a[76:79], v[68:71], v[0:3]\n\t"
      "v_cvt_pk_f16_f32 v105, v6, v7 clamp\n\t"
      "v_pk_mul_f16 v49, v81, v97\n\t"
      "v_mfma_f32_16x16x32_f16 v[4:7], a[80:83], v[56:59], v[28:31]\n\t"
      "v_pk_fmac_f16 v114, v104, v126\n\t"
      "v_fma_f32 v203, v203, v108, -v210\n\t"
      "v_mfma_f32_16x16x32_f16 v[4:7], a[84:87], v[60:63], v[4:7]\n\t"
      "v_pk_fmac_f16 v115, v105, v127\n\t"
      "v_pk_mul_f16 v50, v82, v98\n\t"
      "v_mfma_f32_16x16x32_f16 v[4:7], a[88:91], v[64:67], v[4:7]\n\t"
      "v_cvt_pk_f16_f32 v104, v0, v1 clamp\n\t"
      "v_fma_f32 v204, v204, v108, -v210\n\t"
      "v_mfma_f32_16x16x32_f16 v[4:7], a[92:95], v[68:71], v[4:7]\n\t"
      "v_cvt_pk_f16_f32 v105, v2, v3 clamp\n\t"
      "v_pk_mul_f16 v51, v83, v99\n\t"
      "v_mfma_f32_16x16x32_f16 v[0:3], a[96:99], v[56:59], v[32:35]\n\t"
      "ds_read_b128 v[96:99], v212 offset:2432\n\t"
      "v_pk_fmac_f16 v112, v104, v128\n\t"
      "v_mfma_f32_16x16x32_f16 v[0:3], a[100:103], v[60:63], v[0:3]\n\t"
      "v_pk_fmac_f16 v113, v105, v129\n\t"
      "v_pk_mul_f16 v52, v84, v100\n\t"
      "v_mfma_f32_16x16x32_f16 v[0:3], a[104:107], v[64:67], v[0:3]\n\t"
      "v_cvt_pk_f16_f32 v104, v4, v5 clamp\n\t"
      "v_fma_f32 v205, v205, v108, -v210\n\t"
      "v_mfma_f32_16x16x32_f16 v[0:3], a[108:111], v[68:71], v[0:3]\n\t"
      "v_cvt_pk_f16_f32 v105, v6, v7 clamp\n\t"
      "v_pk_mul_f16 v53, v85, v101\n\t"
      "v_mfma_f32_16x16x32_f16 v[4:7], a[112:115], v[56:59], v[36:39]\n\t"
      "v_pk_fmac_f16 v114, v104, v130\n\t"
      "v_pk_mul_f16 v54, v86, v102\n\t"
      "v_mfma_f32_16x16x32_f16 v[4:7], a[116:119], v[60:63], v[4:7]\n\t"
      "v_pk_fmac_f16 v115, v105, v131\n\t"
      "v_pk_mul_f16 v55, v87, v103\n\t"
      "v_mfma_f32_16x16x32_f16 v[4:7], a[120:123], v[64:67], v[4:7]\n\t"
      "ds_read_b128 v[100:103], v212 offset:2496\n\t"
      "v_cvt_pk_f16_f32 v104, v0, v1 clamp\n\t"
      "v_mfma_f32_16x16x32_f16 v[4:7], a[124:127], v[68:71], v[4:7]\n\t"
      "v_cvt_pk_f16_f32 v105, v2, v3 clamp\n\t"
      "v_fma_f32 v206, v206, v108, -v210\n\t"
      "v_mfma_f32_16x16x32_f16 v[0:3], a[0:3], v[40:43], v[8:11]\n\t"
      "v_pk_fmac_f16 v112, v104, v132\n\t"
      "v_exp_f32 v202, v202\n\t"
      "v_mfma_f32_16x16x32_f16 v[0:3], a[4:7], v[44:47], v[0:3]\n\t"
      "s_waitcnt lgkmcnt(2)\n\t"
      "v_pk_fmac_f16 v113, v105, v133\n\t"
      "v_pk_mul_f16 v56, v72, v88\n\t"
      "v_mfma_f32_16x16x32_f16 v[0:3], a[8:11], v[48:51], v[0:3]\n\t"
      "v_cvt_pk_f16_f32 v104, v4, v5 clamp\n\t"
      "v_exp_f32 v203, v203\n\t"
      "v_mfma_f32_16x16x32_f16 v[0:3], a[12:15], v[52:55], v[0:3]\n\t"
      "v_cvt_pk_f16_f32 v105, v6, v7 clamp\n\t"
      "v_pk_mul_f16 v57, v73, v89\n\t"
      "v_mfma_f32_16x16x32_f16 v[4:7], a[16:19], v[40:43], v[12:15]\n\t"
      "v_pk_fmac_f16 v114, v104, v134\n\t"
      "v_exp_f32 v204, v204\n\t"
      "v_mfma_f32_16x16x32_f16 v[4:7], a[20:23], v[44:47], v[4:7]\n\t"
      "v_pk_fmac_f16 v115, v105, v135\n\t"
      "v_pk_mul_f16 v58, v74, v90\n\t"
      "v_mfma_f32_16x16x32_f16 v[4:7], a[24:27], v[48:51], v[4:7]\n\t"
      "v_cvt_pk_f16_f32 v104, v0, v1 clamp\n\t"
      "v_exp_f32 v205, v205\n\t"
      "v_mfma_f32_16x16x32_f16 v[4:7], a[28:31], v[52:55], v[4:7]\n\t"
      "v_cvt_pk_f16_f32 v105, v2, v3 clamp\n\t"
      "v_pk_mul_f16 v59, v75, v91\n\t"
      "v_mfma_f32_16x16x32_f16 v[116:119], a[140:143], v[112:115], v[116:119]\n\t"
      "ds_read_b128 v[88:91], v212 offset:3456\n\t"
      "v_exp_f32 v206, v206\n\t"
      "v_mfma_f32_16x16x32_f16 v[0:3], a[32:35], v[40:43], v[16:19]\n\t"
      "v_pk_mul_f16 v112, v104, v120\n\t"
      "v_add_f32 v208, v202, v203\n\t"
      "v_mfma_f32_16x16x32_f16 v[0:3], a[36:39], v[44:47], v[0:3]\n\t"
      "v_pk_mul_f16 v113, v105, v121\n\t"
      "v_pk_mul_f16 v60, v76, v92\n\t"
      "v_mfma_f32_16x16x32_f16 v[0:3], a[40:43], v[48:51], v[0:3]\n\t"
      "v_cvt_pk_f16_f32 v104, v4, v5 clamp\n\t"
      "v_mul_f32 v209, v202, v196\n\t"
      "v_mfma_f32_16x16x32_f16 v[0:3], a[44:47], v[52:55], v[0:3]\n\t"
      "v_cvt_pk_f16_f32 v105, v6, v7 clamp\n\t"
      "v_pk_mul_f16 v61, v77, v93\n\t"
      "v_mfma_f32_16x16x32_f16 v[4:7], a[48:51], v[40:43], v[20:23]\n\t"
      "ds_write_b32 v218, v116 offset:256\n\t"
      "v_pk_mul_f16 v114, v104, v122\n\t"
      "v_mfma_f32_16x16x32_f16 v[4:7], a[52:55], v[44:47], v[4:7]\n\t"
      "v_pk_mul_f16 v115, v105, v123\n\t"
      "v_pk_mul_f16 v62, v78, v94\n\t"
      "v_mfma_f32_16x16x32_f16 v[4:7], a[56:59], v[48:51], v[4:7]\n\t"
      "v_cvt_pk_f16_f32 v104, v0, v1 clamp\n\t"
      "v_fmac_f32 v209, v203, v197\n\t"
      "v_mfma_f32_16x16x32_f16 v[4:7], a[60:63], v[52:55], v[4:7]\n\t"
      "v_cvt_pk_f16_f32 v105, v2, v3 clamp\n\t"
      "v_pk_mul_f16 v63, v79, v95\n\t"
      "v_mfma_f32_16x16x32_f16 v[0:3], a[64:67], v[40:43], v[24:27]\n\t"
      "ds_read_b128 v[92:95], v212 offset:3520\n\t"
      "v_pk_fmac_f16 v112, v104, v124\n\t"
      "v_mfma_f32_16x16x32_f16 v[0:3], a[68:71], v[44:47], v[0:3]\n\t"
      "s_waitcnt lgkmcnt(3)\n\t"
      "v_pk_fmac_f16 v113, v105, v125\n\t"
      "v_pk_mul_f16 v64, v80, v96\n\t"
      "v_mfma_f32_16x16x32_f16 v[0:3], a[72:75], v[48:51], v[0:3]\n\t"
      "v_cvt_pk_f16_f32 v104, v4, v5 clamp\n\t"
      "v_add_f32 v208, v208, v204\n\t"
      "v_mfma_f32_16x16x32_f16 v[0:3], a[76:79], v[52:55], v[0:3]\n\t"
      "v_cvt_pk_f16_f32 v105, v6, v7 clamp\n\t"
      "v_pk_mul_f16 v65, v81, v97\n\t"
      "v_mfma_f32_16x16x32_f16 v[4:7], a[80:83], v[40:43], v[28:31]\n\t"
      "v_pk_fmac_f16 v114, v104, v126\n\t"
      "v_fmac_f32 v209, v204, v198\n\t"
      "v_mfma_f32_16x16x32_f16 v[4:7], a[84:87], v[44:47], v[4:7]\n\t"
      "v_pk_fmac_f16 v115, v105, v127\n\t"
      "v_pk_mul_f16 v66, v82, v98\n\t"
      "v_mfma_f32_16x16x32_f16 v[4:7], a[88:91], v[48:51], v[4:7]\n\t"
      "v_cvt_pk_f16_f32 v104, v0, v1 clamp\n\t"
      "v_add_f32 v208, v208, v205\n\t"
      "v_mfma_f32_16x16x32_f16 v[4:7], a[92:95], v[52:55], v[4:7]\n\t"
      "v_cvt_pk_f16_f32 v105, v2, v3 clamp\n\t"
      "v_pk_mul_f16 v67, v83, v99\n\t"
      "v_mfma_f32_16x16x32_f16 v[0:3], a[96:99], v[40:43], v[32:35]\n\t"
      "ds_read_b128 v[96:99], v212 offset:3584\n\t"
      "v_pk_fmac_f16 v112, v104, v128\n\t"
      "v_mfma_f32_16x16x32_f16 v[0:3], a[100:103], v[44:47], v[0:3]\n\t"
      "v_pk_fmac_f16 v113, v105, v129\n\t"
      "v_pk_mul_f16 v68, v84, v100\n\t"
      "v_mfma_f32_16x16x32_f16 v[0:3], a[104:107], v[48:51], v[0:3]\n\t"
      "v_cvt_pk_f16_f32 v104, v4, v5 clamp\n\t"
      "v_fmac_f32 v209, v205, v199\n\t"
      "v_mfma_f32_16x16x32_f16 v[0:3], a[108:111], v[52:55], v[0:3]\n\t"
      "v_cvt_pk_f16_f32 v105, v6, v7 clamp\n\t"
      "v_pk_mul_f16 v69, v85, v101\n\t"
      "v_mfma_f32_16x16x32_f16 v[4:7], a[112:115], v[40:43], v[36:39]\n\t"
      "v_pk_fmac_f16 v114, v104, v130\n\t"
      "v_pk_mul_f16 v70, v86, v102\n\t"
      "v_mfma_f32_16x16x32_f16 v[4:7], a[116:119], v[44:47], v[4:7]\n\t"
      "v_pk_fmac_f16 v115, v105, v131\n\t"
      "v_pk_mul_f16 v71, v87, v103\n\t"
      "v_mfma_f32_16x16x32_f16 v[4:7], a[120:123], v[48:51], v[4:7]\n\t"
      "ds_read_b128 v[100:103], v212 offset:3648\n\t"
      "v_cvt_pk_f16_f32 v104, v0, v1 clamp\n\t"
      "v_mfma_f32_16x16x32_f16 v[4:7], a[124:127], v[52:55], v[4:7]\n\t"
      "v_cvt_pk_f16_f32 v105, v2, v3 clamp\n\t"
      "v_add_f32 v208, v208, v206\n\t"
      "v_mfma_f32_16x16x32_f16 v[0:3], a[0:3], v[56:59], v[8:11]\n\t"
      "v_pk_fmac_f16 v112, v104, v132\n\t"
      "v_fmac_f32 v209, v206, v200\n\t"
      "v_mfma_f32_16x16x32_f16 v[0:3], a[4:7], v[60:63], v[0:3]\n\t"
      "s_waitcnt lgkmcnt(2)\n\t"
      "v_pk_fmac_f16 v113, v105, v133\n\t"
      "v_pk_mul_f16 v40, v72, v88\n\t"
      "v_mfma_f32_16x16x32_f16 v[0:3], a[8:11], v[64:67], v[0:3]\n\t"
      "v_cvt_pk_f16_f32 v104, v4, v5 clamp\n\t"
      "v_add_f32_dpp v208, v208, v208 row_shr:1 row_mask:0xf bank_mask:0xf\n\t"
      "v_mfma_f32_16x16x32_f16 v[0:3], a[12:15], v[68:71], v[0:3]\n\t"
      "v_cvt_pk_f16_f32 v105, v6, v7 clamp\n\t"
      "v_pk_mul_f16 v41, v73, v89\n\t"
      "v_mfma_f32_16x16x32_f16 v[4:7], a[16:19], v[56:59], v[12:15]\n\t"
      "v_pk_fmac_f16 v114, v104, v134\n\t"
      "v_add_f32_dpp v209, v209, v209 row_shr:1 row_mask:0xf bank_mask:0xf\n\t"
      "v_mfma_f32_16x16x32_f16 v[4:7], a[20:23], v[60:63], v[4:7]\n\t"
      "v_pk_fmac_f16 v115, v105, v135\n\t"
      "v_pk_mul_f16 v42, v74, v90\n\t"
      "v_mfma_f32_16x16x32_f16 v[4:7], a[24:27], v[64:67], v[4:7]\n\t"
      "v_cvt_pk_f16_f32 v104, v0, v1 clamp\n\t"
      "v_add_f32_dpp v208, v208, v208 row_shr:2 row_mask:0xf bank_mask:0xf\n\t"
      "v_mfma_f32_16x16x32_f16 v[4:7], a[28:31], v[68:71], v[4:7]\n\t"
      "v_cvt_pk_f16_f32 v105, v2, v3 clamp\n\t"
      "v_pk_mul_f16 v43, v75, v91\n\t"
      "v_mfma_f32_16x16x32_f16 v[116:119], a[128:131], v[112:115], 0\n\t"
      "ds_read_b128 v[88:91], v212 offset:0\n\t"
      "v_add_f32_dpp v209, v209, v209 row_shr:2 row_mask:0xf bank_mask:0xf\n\t"
      "v_mfma_f32_16x16x32_f16 v[0:3], a[32:35], v[56:59], v[16:19]\n\t"
      "ds_read_b128 v[72:75], v213 offset:5760\n\t"
      "v_pk_mul_f16 v112, v104, v120\n\t"
      "v_mfma_f32_16x16x32_f16 v[0:3], a[36:39], v[60:63], v[0:3]\n\t"
      "v_pk_mul_f16 v113, v105, v121\n\t"
      "v_pk_mul_f16 v44, v76, v92\n\t"
      "v_mfma_f32_16x16x32_f16 v[0:3], a[40:43], v[64:67], v[0:3]\n\t"
      "v_cvt_pk_f16_f32 v104, v4, v5 clamp\n\t"
      "v_add_f32_dpp v208, v208, v208 row_shr:4 row_mask:0xf bank_mask:0xf\n\t"
      "v_mfma_f32_16x16x32_f16 v[0:3], a[44:47], v[68:71], v[0:3]\n\t"
      "v_cvt_pk_f16_f32 v105, v6, v7 clamp\n\t"
      "v_pk_mul_f16 v45, v77, v93\n\t"
      "v_mfma_f32_16x16x32_f16 v[4:7], a[48:51], v[56:59], v[20:23]\n\t"
      "v_pk_mul_f16 v114, v104, v122\n\t"
      "v_add_f32_dpp v209, v209, v209 row_shr:4 row_mask:0xf bank_mask:0xf\n\t"
      "v_mfma_f32_16x16x32_f16 v[4:7], a[52:55], v[60:63], v[4:7]\n\t"
      "v_pk_mul_f16 v115, v105, v123\n\t"
      "v_pk_mul_f16 v46, v78, v94\n\t"
      "v_mfma_f32_16x16x32_f16 v[4:7], a[56:59], v[64:67], v[4:7]\n\t"
      "v_cvt_pk_f16_f32 v104, v0, v1 clamp\n\t"
      "v_add_f32_dpp v208, v208, v208 row_shr:8 row_mask:0xf bank_mask:0xf\n\t"
      "v_mfma_f32_16x16x32_f16 v[4:7], a[60:63], v[68:71], v[4:7]\n\t"
      "v_cvt_pk_f16_f32 v105, v2, v3 clamp\n\t"
      "v_pk_mul_f16 v47, v79, v95\n\t"
      "v_mfma_f32_16x16x32_f16 v[0:3], a[64:67], v[56:59], v[24:27]\n\t"
      "ds_read_b128 v[92:95], v212 offset:64\n\t"
      "v_pk_fmac_f16 v112, v104, v124\n\t"
      "v_mfma_f32_16x16x32_f16 v[0:3], a[68:71], v[60:63], v[0:3]\n\t"
      "s_waitcnt lgkmcnt(3)\n\t"
      "ds_read_b128 v[76:79], v213 offset:5824\n\t"
      "v_pk_fmac_f16 v113, v105, v125\n\t"
      "v_pk_mul_f16 v48, v80, v96\n\t"
      "v_mfma_f32_16x16x32_f16 v[0:3], a[72:75], v[64:67], v[0:3]\n\t"
      "v_cvt_pk_f16_f32 v104, v4, v5 clamp\n\t"
      "v_add_f32_dpp v209, v209, v209 row_shr:8 row_mask:0xf bank_mask:0xf\n\t"
      "v_mfma_f32_16x16x32_f16 v[0:3], a[76:79], v[68:71], v[0:3]\n\t"
      "v_cvt_pk_f16_f32 v105, v6, v7 clamp\n\t"
      "v_pk_mul_f16 v49, v81, v97\n\t"
      "v_mfma_f32_16x16x32_f16 v[4:7], a[80:83], v[56:59], v[28:31]\n\t"
      "v_pk_fmac_f16 v114, v104, v126\n\t"
      "v_add_f32_dpp v208, v208, v208 row_bcast:15 row_mask:0xa bank_mask:0xf\n\t"
      "v_mfma_f32_16x16x32_f16 v[4:7], a[84:87], v[60:63], v[4:7]\n\t"
      "v_pk_fmac_f16 v115, v105, v127\n\t"
      "v_pk_mul_f16 v50, v82, v98\n\t"
      "v_mfma_f32_16x16x32_f16 v[4:7], a[88:91], v[64:67], v[4:7]\n\t"
      "v_cvt_pk_f16_f32 v104, v0, v1 clamp\n\t"
      "v_add_f32_dpp v209, v209, v209 row_bcast:15 row_mask:0xa bank_mask:0xf\n\t"
      "v_mfma_f32_16x16x32_f16 v[4:7], a[92:95], v[68:71], v[4:7]\n\t"
      "v_cvt_pk_f16_f32 v105, v2, v3 clamp\n\t"
      "v_pk_mul_f16 v51, v83, v99\n\t"
      "v_mfma_f32_16x16x32_f16 v[0:3], a[96:99], v[56:59], v[32:35]\n\t"
      "ds_read_b128 v[96:99], v212 offset:128\n\t"
      "v_pk_fmac_f16 v112, v104, v128\n\t"
      "v_mfma_f32_16x16x32_f16 v[0:3], a[100:103], v[60:63], v[0:3]\n\t"
      "ds_read_b128 v[80:83], v213 offset:5888\n\t"
      "v_pk_fmac_f16 v113, v105, v129\n\t"
      "v_pk_mul_f16 v52, v84, v100\n\t"
      "v_mfma_f32_16x16x32_f16 v[0:3], a[104:107], v[64:67], v[0:3]\n\t"
      "v_cvt_pk_f16_f32 v104, v4, v5 clamp\n\t"
      "v_add_f32_dpp v208, v208, v208 row_bcast:31 row_mask:0xc bank_mask:0xf\n\t"
      "v_mfma_f32_16x16x32_f16 v[0:3], a[108:111], v[68:71], v[0:3]\n\t"
      "v_cvt_pk_f16_f32 v105, v6, v7 clamp\n\t"
      "v_pk_mul_f16 v53, v85, v101\n\t"
      "v_mfma_f32_16x16x32_f16 v[4:7], a[112:115], v[56:59], v[36:39]\n\t"
      "v_pk_fmac_f16 v114, v104, v130\n\t"
      "v_pk_mul_f16 v54, v86, v102\n\t"
      "v_mfma_f32_16x16x32_f16 v[4:7], a[116:119], v[60:63], v[4:7]\n\t"
      "v_pk_fmac_f16 v115, v105, v131\n\t"
      "v_pk_mul_f16 v55, v87, v103\n\t"
      "v_mfma_f32_16x16x32_f16 v[4:7], a[120:123], v[64:67], v[4:7]\n\t"
      "ds_read_b128 v[100:103], v212 offset:192\n\t"
      "v_cvt_pk_f16_f32 v104, v0, v1 clamp\n\t"
      "v_mfma_f32_16x16x32_f16 v[4:7], a[124:127], v[68:71], v[4:7]\n\t"
      "ds_read_b128 v[84:87], v213 offset:5952\n\t"
      "v_cvt_pk_f16_f32 v105, v2, v3 clamp\n\t"
      "v_mfma_f32_16x16x32_f16 v[0:3], a[0:3], v[40:43], v[8:11]\n\t"
      "v_pk_fmac_f16 v112, v104, v132\n\t"
      "v_add_f32_dpp v209, v209, v209 row_bcast:31 row_mask:0xc bank_mask:0xf\n\t"
      "v_mfma_f32_16x16x32_f16 v[0:3], a[4:7], v[44:47], v[0:3]\n\t"
      "s_waitcnt lgkmcnt(4)\n\t"
      "v_pk_fmac_f16 v113, v105, v133\n\t"
      "v_pk_mul_f16 v56, v72, v88\n\t"
      "v_mfma_f32_16x16x32_f16 v[0:3], a[8:11], v[48:51], v[0:3]\n\t"
      "v_cvt_pk_f16_f32 v104, v4, v5 clamp\n\t"
      "v_readlane_b32 s55, v208, 63\n\t"
      "v_mfma_f32_16x16x32_f16 v[0:3], a[12:15], v[52:55], v[0:3]\n\t"
      "v_cvt_pk_f16_f32 v105, v6, v7 clamp\n\t"
      "v_pk_mul_f16 v57, v73, v89\n\t"
      "v_mfma_f32_16x16x32_f16 v[4:7], a[16:19], v[40:43], v[12:15]\n\t"
      "v_pk_fmac_f16 v114, v104, v134\n\t"
      "v_readlane_b32 s56, v209, 63\n\t"
      "v_mfma_f32_16x16x32_f16 v[4:7], a[20:23], v[44:47], v[4:7]\n\t"
      "v_pk_fmac_f16 v115, v105, v135\n\t"
      "v_pk_mul_f16 v58, v74, v90\n\t"
      "v_mfma_f32_16x16x32_f16 v[4:7], a[24:27], v[48:51], v[4:7]\n\t"
      "v_cvt_pk_f16_f32 v104, v0, v1 clamp\n\t"
      "v_writelane_b32 v245, s55, m0\n\t"
      "v_mfma_f32_16x16x32_f16 v[4:7], a[28:31], v[52:55], v[4:7]\n\t"
      "v_cvt_pk_f16_f32 v105, v2, v3 clamp\n\t"
      "v_pk_mul_f16 v59, v75, v91\n\t"
      "v_mfma_f32_16x16x32_f16 v[116:119], a[132:135], v[112:115], v[116:119]\n\t"
      "ds_read_b128 v[88:91], v212 offset:1152\n\t"
      "v_writelane_b32 v246, s56, m0\n\t"
      "v_mfma_f32_16x16x32_f16 v[0:3], a[32:35], v[40:43], v[16:19]\n\t"
      "s_lshl_b32 m0, s48, 2\n\t"
      "v_pk_mul_f16 v112, v104, v120\n\t"
      "v_lshrrev_b32 v106, 16, v243\n\t"
      "v_mfma_f32_16x16x32_f16 v[0:3], a[36:39], v[44:47], v[0:3]\n\t"
      "v_pk_mul_f16 v113, v105, v121\n\t"
      "v_pk_mul_f16 v60, v76, v92\n\t"
      "v_mfma_f32_16x16x32_f16 v[0:3], a[40:43], v[48:51], v[0:3]\n\t"
      "v_cvt_pk_f16_f32 v104, v4, v5 clamp\n\t"
      "v_and_b32 v243, 0xffff, v243\n\t"
      "v_mfma_f32_16x16x32_f16 v[0:3], a[44:47], v[52:55], v[0:3]\n\t"
      "v_cvt_pk_f16_f32 v105, v6, v7 clamp\n\t"
      "v_pk_mul_f16 v61, v77, v93\n\t"
      "v_mfma_f32_16x16x32_f16 v[4:7], a[48:51], v[40:43], v[20:23]\n\t"
      "v_pk_mul_f16 v114, v104, v122\n\t"
      "v_max_u32 v243, v243, v106\n\t"
      "v_mfma_f32_16x16x32_f16 v[4:7], a[52:55], v[44:47], v[4:7]\n\t"
      "v_pk_mul_f16 v115, v105, v123\n\t"
      "v_pk_mul_f16 v62, v78, v94\n\t"
      "v_mfma_f32_16x16x32_f16 v[4:7], a[56:59], v[48:51], v[4:7]\n\t"
      "v_cvt_pk_f16_f32 v104, v0, v1 clamp\n\t"
      "v_max_u32_dpp v243, v243, v243 row_shr:1 row_mask:0xf bank_mask:0xf\n\t"
      "v_mfma_f32_16x16x32_f16 v[4:7], a[60:63], v[52:55], v[4:7]\n\t"
      "v_cvt_pk_f16_f32 v105, v2, v3 clamp\n\t"
      "v_pk_mul_f16 v63, v79, v95\n\t"
      "v_mfma_f32_16x16x32_f16 v[0:3], a[64:67], v[40:43], v[24:27]\n\t"
      "ds_read_b128 v[92:95], v212 offset:1216\n\t"
      "v_pk_fmac_f16 v112, v104, v124\n\t"
      "v_mfma_f32_16x16x32_f16 v[0:3], a[68:71], v[44:47], v[0:3]\n\t"
      "s_waitcnt lgkmcnt(2)\n\t"
      "v_pk_fmac_f16 v113, v105, v125\n\t"
      "v_pk_mul_f16 v64, v80, v96\n\t"
      "v_mfma_f32_16x16x32_f16 v[0:3], a[72:75], v[48:51], v[0:3]\n\t"
      "ds_read_b128 v[164:167], v216 offset:0\n\t"
      "v_cvt_pk_f16_f32 v104, v4, v5 clamp\n\t"
      "v_mfma_f32_16x16x32_f16 v[0:3], a[76:79], v[52:55], v[0:3]\n\t"
      "v_cvt_pk_f16_f32 v105, v6, v7 clamp\n\t"
      "v_pk_mul_f16 v65, v81, v97\n\t"
      "v_mfma_f32_16x16x32_f16 v[4:7], a[80:83], v[40:43], v[28:31]\n\t"
      "ds_read_b128 v[168:171], v217 offset:0\n\t"
      "v_pk_fmac_f16 v114, v104, v126\n\t"
      "v_mfma_f32_16x16x32_f16 v[4:7], a[84:87], v[44:47], v[4:7]\n\t"
      "v_pk_fmac_f16 v115, v105, v127\n\t"
      "v_pk_mul_f16 v66, v82, v98\n\t"
      "v_mfma_f32_16x16x32_f16 v[4:7], a[88:91], v[48:51], v[4:7]\n\t"
      "ds_read_b128 v[180:183], v225 offset:0\n\t"
      "v_cvt_pk_f16_f32 v104, v0, v1 clamp\n\t"
      "v_mfma_f32_16x16x32_f16 v[4:7], a[92:95], v[52:55], v[4:7]\n\t"
      "v_cvt_pk_f16_f32 v105, v2, v3 clamp\n\t"
      "v_pk_mul_f16 v67, v83, v99\n\t"
      "v_mfma_f32_16x16x32_f16 v[0:3], a[96:99], v[40:43], v[32:35]\n\t"
      "ds_read_b128 v[96:99], v212 offset:1280\n\t"
      "v_pk_fmac_f16 v112, v104, v128\n\t"
      "v_mfma_f32_16x16x32_f16 v[0:3], a[100:103], v[44:47], v[0:3]\n\t"
      "v_pk_fmac_f16 v113, v105, v129\n\t"
      "v_pk_mul_f16 v68, v84, v100\n\t"
      "v_mfma_f32_16x16x32_f16 v[0:3], a[104:107], v[48:51], v[0:3]\n\t"
      "v_cvt_pk_f16_f32 v104, v4, v5 clamp\n\t"
      "v_max_u32_dpp v243, v243, v243 row_shr:2 row_mask:0xf bank_mask:0xf\n\t"
      "v_mfma_f32_16x16x32_f16 v[0:3], a[108:111], v[52:55], v[0:3]\n\t"
      "v_cvt_pk_f16_f32 v105, v6, v7 clamp\n\t"
      "v_pk_mul_f16 v69, v85, v101\n\t"
      "v_mfma_f32_16x16x32_f16 v[4:7], a[112:115], v[40:43], v[36:39]\n\t"
      "v_pk_fmac_f16 v114, v104, v130\n\t"
      "v_pk_mul_f16 v70, v86, v102\n\t"
      "v_mfma_f32_16x16x32_f16 v[4:7], a[116:119], v[44:47], v[4:7]\n\t"
      "v_pk_fmac_f16 v115, v105, v131\n\t"
      "v_pk_mul_f16 v71, v87, v103\n\t"
      "v_mfma_f32_16x16x32_f16 v[4:7], a[120:123], v[48:51], v[4:7]\n\t"
      "ds_read_b128 v[100:103], v212 offset:1344\n\t"
      "v_cvt_pk_f16_f32 v104, v0, v1 clamp\n\t"
      "v_mfma_f32_16x16x32_f16 v[4:7], a[124:127], v[52:55], v[4:7]\n\t"
      "v_cvt_pk_f16_f32 v105, v2, v3 clamp\n\t"
      "v_max_u32_dpp v243, v243, v243 row_shr:4 row_mask:0xf bank_mask:0xf\n\t"
      "v_mfma_f32_16x16x32_f16 v[0:3], a[0:3], v[56:59], v[8:11]\n\t"
      "s_waitcnt lgkmcnt(2)\n\t"
      "v_pk_fmac_f16 v112, v104, v132\n\t"
      "v_pk_mul_f16 v172, v164, v180\n\t"
      "v_mfma_f32_16x16x32_f16 v[0:3], a[4:7], v[60:63], v[0:3]\n\t"
      "v_pk_fmac_f16 v113, v105, v133\n\t"
      "v_pk_mul_f16 v40, v72, v88\n\t"
      "v_mfma_f32_16x16x32_f16 v[0:3], a[8:11], v[64:67], v[0:3]\n\t"
      "v_cvt_pk_f16_f32 v104, v4, v5 clamp\n\t"
      "v_pk_mul_f16 v173, v165, v181\n\t"
      "v_mfma_f32_16x16x32_f16 v[0:3], a[12:15], v[68:71], v[0:3]\n\t"
      "v_cvt_pk_f16_f32 v105, v6, v7 clamp\n\t"
      "v_pk_mul_f16 v41, v73, v89\n\t"
      "v_mfma_f32_16x16x32_f16 v[4:7], a[16:19], v[56:59], v[12:15]\n\t"
      "v_pk_fmac_f16 v114, v104, v134\n\t"
      "v_pk_mul_f16 v174, v166, v182\n\t"
      "v_mfma_f32_16x16x32_f16 v[4:7], a[20:23], v[60:63], v[4:7]\n\t"
      "v_pk_fmac_f16 v115, v105, v135\n\t"
      "v_pk_mul_f16 v42, v74, v90\n\t"
      "v_mfma_f32_16x16x32_f16 v[4:7], a[24:27], v[64:67], v[4:7]\n\t"
      "v_cvt_pk_f16_f32 v104, v0, v1 clamp\n\t"
      "v_pk_mul_f16 v175, v167, v183\n\t"
      "v_mfma_f32_16x16x32_f16 v[4:7], a[28:31], v[68:71], v[4:7]\n\t"
      "v_cvt_pk_f16_f32 v105, v2, v3 clamp\n\t"
      "v_pk_mul_f16 v43, v75, v91\n\t"
      "v_mfma_f32_16x16x32_f16 v[116:119], a[136:139], v[112:115], v[116:119]\n\t"
      "ds_read_b128 v[88:91], v212 offset:2304\n\t"
      "v_pk_mul_f16 v176, v168, v180\n\t"
      "v_mfma_f32_16x16x32_f16 v[0:3], a[32:35], v[56:59], v[16:19]\n\t"
      "v_pk_mul_f16 v112, v104, v120\n\t"
      "v_pk_mul_f16 v177, v169, v181\n\t"
      "v_mfma_f32_16x16x32_f16 v[0:3], a[36:39], v[60:63], v[0:3]\n\t"
      "v_pk_mul_f16 v113, v105, v121\n\t"
      "v_pk_mul_f16 v44, v76, v92\n\t"
      "v_mfma_f32_16x16x32_f16 v[0:3], a[40:43], v[64:67], v[0:3]\n\t"
      "v_cvt_pk_f16_f32 v104, v4, v5 clamp\n\t"
      "v_pk_mul_f16 v178, v170, v182\n\t"
      "v_mfma_f32_16x16x32_f16 v[0:3], a[44:47], v[68:71], v[0:3]\n\t"
      "v_cvt_pk_f16_f32 v105, v6, v7 clamp\n\t"
      "v_pk_mul_f16 v45, v77, v93\n\t"
      "v_mfma_f32_16x16x32_f16 v[4:7], a[48:51], v[56:59], v[20:23]\n\t"
      "v_pk_mul_f16 v114, v104, v122\n\t"
      "v_pk_mul_f16 v179, v171, v183\n\t"
      "v_mfma_f32_16x16x32_f16 v[4:7], a[52:55], v[60:63], v[4:7]\n\t"
      "v_pk_mul_f16 v115, v105, v123\n\t"
      "v_pk_mul_f16 v46, v78, v94\n\t"
      "v_mfma_f32_16x16x32_f16 v[4:7], a[56:59], v[64:67], v[4:7]\n\t"
      "v_cvt_pk_f16_f32 v104, v0, v1 clamp\n\t"
      "v_max_u32_dpp v243, v243, v243 row_shr:8 row_mask:0xf bank_mask:0xf\n\t"
      "v_mfma_f32_16x16x32_f16 v[4:7], a[60:63], v[68:71], v[4:7]\n\t"
      "v_cvt_pk_f16_f32 v105, v2, v3 clamp\n\t"
      "v_pk_mul_f16 v47, v79, v95\n\t"
      "v_mfma_f32_16x16x32_f16 v[0:3], a[64:67], v[56:59], v[24:27]\n\t"
      "ds_read_b128 v[92:95], v212 offset:2368\n\t"
      "v_pk_fmac_f16 v112, v104, v124\n\t"
      "v_mfma_f32_16x16x32_f16 v[0:3], a[68:71], v[60:63], v[0:3]\n\t"
      "s_waitcnt lgkmcnt(2)\n\t"
      "v_pk_fmac_f16 v113, v105, v125\n\t"
      "v_pk_mul_f16 v48, v80, v96\n\t"
      "v_mfma_f32_16x16x32_f16 v[0:3], a[72:75], v[64:67], v[0:3]\n\t"
      "v_cvt_pk_f16_f32 v104, v4, v5 clamp\n\t"
      "v_max_u32_dpp v243, v243, v243 row_bcast:15 row_mask:0xa bank_mask:0xf\n\t"
      "v_mfma_f32_16x16x32_f16 v[184:187], v[172:175], v[164:167], 0\n\t"
      "s_nop 0\n\t"
      "v_max_u32_dpp v243, v243, v243 row_bcast:31 row_mask:0xc bank_mask:0xf\n\t"
      "v_add_u32 v220, s45, v220\n\t"
      "v_mfma_f32_16x16x32_f16 v[0:3], a[76:79], v[68:71], v[0:3]\n\t"
      "v_cvt_pk_f16_f32 v105, v6, v7 clamp\n\t"
      "v_pk_mul_f16 v49, v81, v97\n\t"
      "v_mfma_f32_16x16x32_f16 v[4:7], a[80:83], v[56:59], v[28:31]\n\t"
      "v_pk_fmac_f16 v114, v104, v126\n\t"
      "v_readlane_b32 s57, v243, 63\n\t"
      "v_mfma_f32_16x16x32_f16 v[188:191], v[172:175], v[168:171], 0\n\t"
      "v_add_u32 v221, s45, v221\n\t"
      "v_add_u32 v222, 1728, v222\n\t"
      "v_mfma_f32_16x16x32_f16 v[4:7], a[84:87], v[60:63], v[4:7]\n\t"
      "v_pk_fmac_f16 v115, v105, v127\n\t"
      "v_pk_mul_f16 v50, v82, v98\n\t"
      "v_mfma_f32_16x16x32_f16 v[4:7], a[88:91], v[64:67], v[4:7]\n\t"
      "v_cvt_pk_f16_f32 v104, v0, v1 clamp\n\t"
      "v_writelane_b32 v247, s57, m0\n\t"
      "v_mfma_f32_16x16x32_f16 v[192:195], v[176:179], v[168:171], 0\n\t"
      "v_add_u32 v223, 1728, v223\n\t"
      "v_mfma_f32_16x16x32_f16 v[4:7], a[92:95], v[68:71], v[4:7]\n\t"
      "v_cvt_pk_f16_f32 v105, v2, v3 clamp\n\t"
      "v_pk_mul_f16 v51, v83, v99\n\t"
      "v_mfma_f32_16x16x32_f16 v[0:3], a[96:99], v[56:59], v[32:35]\n\t"
      "ds_read_b128 v[96:99], v212 offset:2432\n\t"
      "v_pk_fmac_f16 v112, v104, v128\n\t"
      "v_mfma_f32_16x16x32_f16 v[0:3], a[100:103], v[60:63], v[0:3]\n\t"
      "v_pk_fmac_f16 v113, v105, v129\n\t"
      "v_pk_mul_f16 v52, v84, v100\n\t"
      "v_mfma_f32_16x16x32_f16 v[0:3], a[104:107], v[64:67], v[0:3]\n\t"
      "ds_read_b128 v[164:167], v216 offset:64\n\t"
      "v_cvt_pk_f16_f32 v104, v4, v5 clamp\n\t"
      "v_mfma_f32_16x16x32_f16 v[0:3], a[108:111], v[68:71], v[0:3]\n\t"
      "v_cvt_pk_f16_f32 v105, v6, v7 clamp\n\t"
      "v_pk_mul_f16 v53, v85, v101\n\t"
      "v_mfma_f32_16x16x32_f16 v[4:7], a[112:115], v[56:59], v[36:39]\n\t"
      "v_pk_fmac_f16 v114, v104, v130\n\t"
      "v_pk_mul_f16 v54, v86, v102\n\t"
      "v_mfma_f32_16x16x32_f16 v[4:7], a[116:119], v[60:63], v[4:7]\n\t"
      "v_pk_fmac_f16 v115, v105, v131\n\t"
      "v_pk_mul_f16 v55, v87, v103\n\t"
      "v_mfma_f32_16x16x32_f16 v[4:7], a[120:123], v[64:67], v[4:7]\n\t"
      "ds_read_b128 v[100:103], v212 offset:2496\n\t"
      "v_cvt_pk_f16_f32 v104, v0, v1 clamp\n\t"
      "v_mfma_f32_16x16x32_f16 v[4:7], a[124:127], v[68:71], v[4:7]\n\t"
      "ds_read_b128 v[168:171], v217 offset:64\n\t"
      "v_cvt_pk_f16_f32 v105, v2, v3 clamp\n\t"
      "v_mfma_f32_16x16x32_f16 v[0:3], a[0:3], v[40:43], v[8:11]\n\t"
      "ds_read_b128 v[180:183], v225 offset:64\n\t"
      "v_pk_fmac_f16 v112, v104, v132\n\t"
      "v_mfma_f32_16x16x32_f16 v[0:3], a[4:7], v[44:47], v[0:3]\n\t"
      "s_waitcnt lgkmcnt(5)\n\t"
      "v_pk_fmac_f16 v113, v105, v133\n\t"
      "v_pk_mul_f16 v56, v72, v88\n\t"
      "v_mfma_f32_16x16x32_f16 v[0:3], a[8:11], v[48:51], v[0:3]\n\t"
      "s_waitcnt lgkmcnt(0)\n\t"
      "v_cvt_pk_f16_f32 v104, v4, v5 clamp\n\t"
      "v_pk_mul_f16 v172, v164, v180\n\t"
      "v_mfma_f32_16x16x32_f16 v[0:3], a[12:15], v[52:55], v[0:3]\n\t"
      "v_cvt_pk_f16_f32 v105, v6, v7 clamp\n\t"
      "v_pk_mul_f16 v57, v73, v89\n\t"
      "v_mfma_f32_16x16x32_f16 v[4:7], a[16:19], v[40:43], v[12:15]\n\t"
      "v_pk_fmac_f16 v114, v104, v134\n\t"
      "v_pk_mul_f16 v173, v165, v181\n\t"
      "v_mfma_f32_16x16x32_f16 v[4:7], a[20:23], v[44:47], v[4:7]\n\t"
      "v_pk_fmac_f16 v115, v105, v135\n\t"
      "v_pk_mul_f16 v58, v74, v90\n\t"
      "v_mfma_f32_16x16x32_f16 v[4:7], a[24:27], v[48:51], v[4:7]\n\t"
      "v_cvt_pk_f16_f32 v104, v0, v1 clamp\n\t"
      "v_pk_mul_f16 v174, v166, v182\n\t"
      "v_mfma_f32_16x16x32_f16 v[4:7], a[28:31], v[52:55], v[4:7]\n\t"
      "v_cvt_pk_f16_f32 v105, v2, v3 clamp\n\t"
      "v_pk_mul_f16 v59, v75, v91\n\t"
      "v_mfma_f32_16x16x32_f16 v[116:119], a[140:143], v[112:115], v[116:119]\n\t"
      "ds_read_b128 v[88:91], v212 offset:3456\n\t"
      "v_pk_mul_f16 v175, v167, v183\n\t"
      "v_mfma_f32_16x16x32_f16 v[0:3], a[32:35], v[40:43], v[16:19]\n\t"
      "v_pk_mul_f16 v112, v104, v120\n\t"
      "v_pk_mul_f16 v176, v168, v180\n\t"
      "v_mfma_f32_16x16x32_f16 v[0:3], a[36:39], v[44:47], v[0:3]\n\t"
      "v_pk_mul_f16 v113, v105, v121\n\t"
      "v_pk_mul_f16 v60, v76, v92\n\t"
      "v_mfma_f32_16x16x32_f16 v[0:3], a[40:43], v[48:51], v[0:3]\n\t"
      "v_cvt_pk_f16_f32 v104, v4, v5 clamp\n\t"
      "v_pk_mul_f16 v177, v169, v181\n\t"
      "v_mfma_f32_16x16x32_f16 v[0:3], a[44:47], v[52:55], v[0:3]\n\t"
      "v_cvt_pk_f16_f32 v105, v6, v7 clamp\n\t"
      "v_pk_mul_f16 v61, v77, v93\n\t"
      "v_mfma_f32_16x16x32_f16 v[4:7], a[48:51], v[40:43], v[20:23]\n\t"
      "ds_write_b32 v218, v116 offset:512\n\t"
      "v_pk_mul_f16 v114, v104, v122\n\t"
      "v_mfma_f32_16x16x32_f16 v[4:7], a[52:55], v[44:47], v[4:7]\n\t"
      "v_pk_mul_f16 v115, v105, v123\n\t"
      "v_pk_mul_f16 v62, v78, v94\n\t"
      "v_mfma_f32_16x16x32_f16 v[4:7], a[56:59], v[48:51], v[4:7]\n\t"
      "v_cvt_pk_f16_f32 v104, v0, v1 clamp\n\t"
      "v_pk_mul_f16 v178, v170, v182\n\t"
      "v_mfma_f32_16x16x32_f16 v[4:7], a[60:63], v[52:55], v[4:7]\n\t"
      "v_cvt_pk_f16_f32 v105, v2, v3 clamp\n\t"
      "v_pk_mul_f16 v63, v79, v95\n\t"
      "v_mfma_f32_16x16x32_f16 v[0:3], a[64:67], v[40:43], v[24:27]\n\t"
      "ds_read_b128 v[92:95], v212 offset:3520\n\t"
      "v_pk_fmac_f16 v112, v104, v124\n\t"
      "v_mfma_f32_16x16x32_f16 v[0:3], a[68:71], v[44:47], v[0:3]\n\t"
      "v_pk_fmac_f16 v113, v105, v125\n\t"
      "v_pk_mul_f16 v64, v80, v96\n\t"
      "v_mfma_f32_16x16x32_f16 v[0:3], a[72:75], v[48:51], v[0:3]\n\t"
      "v_cvt_pk_f16_f32 v104, v4, v5 clamp\n\t"
      "v_pk_mul_f16 v179, v171, v183\n\t"
      "v_mfma_f32_16x16x32_f16 v[184:187], v[172:175], v[164:167], v[184:187]\n\t"
      "s_waitcnt vmcnt(9)\n\t"
      "v_and_b32 v106, s36, v136\n\t"
      "v_and_b32 v107, s36, v137\n\t"
      "v_mfma_f32_16x16x32_f16 v[0:3], a[76:79], v[52:55], v[0:3]\n\t"
      "v_cvt_pk_f16_f32 v105, v6, v7 clamp\n\t"
      "v_pk_mul_f16 v65, v81, v97\n\t"
      "v_mfma_f32_16x16x32_f16 v[4:7], a[80:83], v[40:43], v[28:31]\n\t"
      "v_pk_fmac_f16 v114, v104, v126\n\t"
      "v_pk_max_u16 v243, v106, v107\n\t"
      "v_mfma_f32_16x16x32_f16 v[188:191], v[172:175], v[168:171], v[188:191]\n\t"
      "v_pk_sub_u16 v106, v106, s44 clamp\n\t"
      "v_and_b32 v136, s37, v136\n\t"
      "v_mfma_f32_16x16x32_f16 v[4:7], a[84:87], v[44:47], v[4:7]\n\t"
      "v_pk_fmac_f16 v115, v105, v127\n\t"
      "v_pk_mul_f16 v66, v82, v98\n\t"
      "v_mfma_f32_16x16x32_f16 v[4:7], a[88:91], v[48:51], v[4:7]\n\t"
      "v_cvt_pk_f16_f32 v104, v0, v1 clamp\n\t"
      "v_pk_sub_u16 v107, v107, s44 clamp\n\t"
      "v_mfma_f32_16x16x32_f16 v[192:195], v[176:179], v[168:171], v[192:195]\n\t"
      "v_and_b32 v137, s37, v137\n\t"
      "v_lshl_or_b32 v136, v106, 3, v136\n\t"
      "v_mfma_f32_16x16x32_f16 v[4:7], a[92:95], v[52:55], v[4:7]\n\t"
      "v_cvt_pk_f16_f32 v105, v2, v3 clamp\n\t"
      "v_pk_mul_f16 v67, v83, v99\n\t"
      "v_mfma_f32_16x16x32_f16 v[0:3], a[96:99], v[40:43], v[32:35]\n\t"
      "ds_read_b128 v[96:99], v212 offset:3584\n\t"
      "v_pk_fmac_f16 v112, v104, v128\n\t"
      "v_mfma_f32_16x16x32_f16 v[0:3], a[100:103], v[44:47], v[0:3]\n\t"
      "v_pk_fmac_f16 v113, v105, v129\n\t"
      "v_pk_mul_f16 v68, v84, v100\n\t"
      "v_mfma_f32_16x16x32_f16 v[0:3], a[104:107], v[48:51], v[0:3]\n\t"
      "ds_read_b128 v[164:167], v216 offset:128\n\t"
      "v_cvt_pk_f16_f32 v104, v4, v5 clamp\n\t"
      "v_mfma_f32_16x16x32_f16 v[0:3], a[108:111], v[52:55], v[0:3]\n\t"
      "v_cvt_pk_f16_f32 v105, v6, v7 clamp\n\t"
      "v_pk_mul_f16 v69, v85, v101\n\t"
      "v_mfma_f32_16x16x32_f16 v[4:7], a[112:115], v[40:43], v[36:39]\n\t"
      "v_pk_fmac_f16 v114, v104, v130\n\t"
      "v_pk_mul_f16 v70, v86, v102\n\t"
      "v_mfma_f32_16x16x32_f16 v[4:7], a[116:119], v[44:47], v[4:7]\n\t"
      "v_pk_fmac_f16 v115, v105, v131\n\t"
      "v_pk_mul_f16 v71, v87, v103\n\t"
      "v_mfma_f32_16x16x32_f16 v[4:7], a[120:123], v[48:51], v[4:7]\n\t"
      "ds_read_b128 v[100:103], v212 offset:3648\n\t"
      "v_cvt_pk_f16_f32 v104, v0, v1 clamp\n\t"
      "v_mfma_f32_16x16x32_f16 v[4:7], a[124:127], v[52:55], v[4:7]\n\t"
      "ds_read_b128 v[168:171], v217 offset:128\n\t"
      "v_cvt_pk_f16_f32 v105, v2, v3 clamp\n\t"
      "v_mfma_f32_16x16x32_f16 v[0:3], a[0:3], v[56:59], v[8:11]\n\t"
      "ds_read_b128 v[180:183], v225 offset:128\n\t"
      "v_pk_fmac_f16 v112, v104, v132\n\t"
      "v_mfma_f32_16x16x32_f16 v[0:3], a[4:7], v[60:63], v[0:3]\n\t"
      "s_waitcnt lgkmcnt(5)\n\t"
      "v_pk_fmac_f16 v113, v105, v133\n\t"
      "v_pk_mul_f16 v40, v72, v88\n\t"
      "v_mfma_f32_16x16x32_f16 v[0:3], a[8:11], v[64:67], v[0:3]\n\t"
      "s_waitcnt lgkmcnt(0)\n\t"
      "v_cvt_pk_f16_f32 v104, v4, v5 clamp\n\t"
      "v_pk_mul_f16 v172, v164, v180\n\t"
      "v_mfma_f32_16x16x32_f16 v[0:3], a[12:15], v[68:71], v[0:3]\n\t"
      "v_cvt_pk_f16_f32 v105, v6, v7 clamp\n\t"
      "v_pk_mul_f16 v41, v73, v89\n\t"
      "v_mfma_f32_16x16x32_f16 v[4:7], a[16:19], v[56:59], v[12:15]\n\t"
      "v_pk_fmac_f16 v114, v104, v134\n\t"
      "v_pk_mul_f16 v173, v165, v181\n\t"
      "v_mfma_f32_16x16x32_f16 v[4:7], a[20:23], v[60:63], v[4:7]\n\t"
      "v_pk_fmac_f16 v115, v105, v135\n\t"
      "v_pk_mul_f16 v42, v74, v90\n\t"
      "v_mfma_f32_16x16x32_f16 v[4:7], a[24:27], v[64:67], v[4:7]\n\t"
      "v_cvt_pk_f16_f32 v104, v0, v1 clamp\n\t"
      "v_pk_mul_f16 v174, v166, v182\n\t"
      "v_mfma_f32_16x16x32_f16 v[4:7], a[28:31], v[68:71], v[4:7]\n\t"
      "v_cvt_pk_f16_f32 v105, v2, v3 clamp\n\t"
      "v_pk_mul_f16 v43, v75, v91\n\t"
      "v_mfma_f32_16x16x32_f16 v[116:119], a[128:131], v[112:115], 0\n\t"
      "ds_read_b128 v[88:91], v212 offset:4608\n\t"
      "v_pk_mul_f16 v175, v167, v183\n\t"
      "v_mfma_f32_16x16x32_f16 v[0:3], a[32:35], v[56:59], v[16:19]\n\t"
      "v_pk_mul_f16 v112, v104, v120\n\t"
      "v_pk_mul_f16 v176, v168, v180\n\t"
      "v_mfma_f32_16x16x32_f16 v[0:3], a[36:39], v[60:63], v[0:3]\n\t"
      "v_pk_mul_f16 v113, v105, v121\n\t"
      "v_pk_mul_f16 v44, v76, v92\n\t"
      "v_mfma_f32_16x16x32_f16 v[0:3], a[40:43], v[64:67], v[0:3]\n\t"
      "v_cvt_pk_f16_f32 v104, v4, v5 clamp\n\t"
      "v_pk_mul_f16 v177, v169, v181\n\t"
      "v_mfma_f32_16x16x32_f16 v[0:3], a[44:47], v[68:71], v[0:3]\n\t"
      "v_cvt_pk_f16_f32 v105, v6, v7 clamp\n\t"
      "v_pk_mul_f16 v45, v77, v93\n\t"
      "v_mfma_f32_16x16x32_f16 v[4:7], a[48:51], v[56:59], v[20:23]\n\t"
      "v_pk_mul_f16 v114, v104, v122\n\t"
      "v_pk_mul_f16 v178, v170, v182\n\t"
      "v_mfma_f32_16x16x32_f16 v[4:7], a[52:55], v[60:63], v[4:7]\n\t"
      "v_pk_mul_f16 v115, v105, v123\n\t"
      "v_pk_mul_f16 v46, v78, v94\n\t"
      "v_mfma_f32_16x16x32_f16 v[4:7], a[56:59], v[64:67], v[4:7]\n\t"
      "v_cvt_pk_f16_f32 v104, v0, v1 clamp\n\t"
      "v_pk_mul_f16 v179, v171, v183\n\t"
      "v_mfma_f32_16x16x32_f16 v[4:7], a[60:63], v[68:71], v[4:7]\n\t"
      "v_cvt_pk_f16_f32 v105, v2, v3 clamp\n\t"
      "v_pk_mul_f16 v47, v79, v95\n\t"
      "v_mfma_f32_16x16x32_f16 v[0:3], a[64:67], v[56:59], v[24:27]\n\t"
      "ds_read_b128 v[92:95], v212 offset:4672\n\t"
      "v_pk_fmac_f16 v112, v104, v124\n\t"
      "v_mfma_f32_16x16x32_f16 v[0:3], a[68:71], v[60:63], v[0:3]\n\t"
      "v_pk_fmac_f16 v113, v105, v125\n\t"
      "v_pk_mul_f16 v48, v80, v96\n\t"
      "v_mfma_f32_16x16x32_f16 v[0:3], a[72:75], v[64:67], v[0:3]\n\t"
      "v_cvt_pk_f16_f32 v104, v4, v5 clamp\n\t"
      "v_lshl_or_b32 v137, v107, 3, v137\n\t"
      "v_mfma_f32_16x16x32_f16 v[184:187], v[172:175], v[164:167], v[184:187]\n\t"
      "v_and_b32 v106, s36, v138\n\t"
      "v_and_b32 v107, s36, v139\n\t"
      "v_mfma_f32_16x16x32_f16 v[0:3], a[76:79], v[68:71], v[0:3]\n\t"
      "v_cvt_pk_f16_f32 v105, v6, v7 clamp\n\t"
      "v_pk_mul_f16 v49, v81, v97\n\t"
      "v_mfma_f32_16x16x32_f16 v[4:7], a[80:83], v[56:59], v[28:31]\n\t"
      "v_pk_fmac_f16 v114, v104, v126\n\t"
      "v_pk_maximum3_f16 v243, v243, v106, v107\n\t"
      "v_mfma_f32_16x16x32_f16 v[188:191], v[172:175], v[168:171], v[188:191]\n\t"
      "v_pk_sub_u16 v106, v106, s44 clamp\n\t"
      "v_and_b32 v138, s37, v138\n\t"
      "v_mfma_f32_16x16x32_f16 v[4:7], a[84:87], v[60:63], v[4:7]\n\t"
      "v_pk_fmac_f16 v115, v105, v127\n\t"
      "v_pk_mul_f16 v50, v82, v98\n\t"
      "v_mfma_f32_16x16x32_f16 v[4:7], a[88:91], v[64:67], v[4:7]\n\t"
      "v_cvt_pk_f16_f32 v104, v0, v1 clamp\n\t"
      "v_pk_sub_u16 v107, v107, s44 clamp\n\t"
      "v_mfma_f32_16x16x32_f16 v[192:195], v[176:179], v[168:171], v[192:195]\n\t"
      "v_and_b32 v139, s37, v139\n\t"
      "v_lshl_or_b32 v138, v106, 3, v138\n\t"
      "v_mfma_f32_16x16x32_f16 v[4:7], a[92:95], v[68:71], v[4:7]\n\t"
      "v_cvt_pk_f16_f32 v105, v2, v3 clamp\n\t"
      "v_pk_mul_f16 v51, v83, v99\n\t"
      "v_mfma_f32_16x16x32_f16 v[0:3], a[96:99], v[56:59], v[32:35]\n\t"
      "ds_read_b128 v[96:99], v212 offset:4736\n\t"
      "v_pk_fmac_f16 v112, v104, v128\n\t"
      "v_mfma_f32_16x16x32_f16 v[0:3], a[100:103], v[60:63], v[0:3]\n\t"
      "v_pk_fmac_f16 v113, v105, v129\n\t"
      "v_pk_mul_f16 v52, v84, v100\n\t"
      "v_mfma_f32_16x16x32_f16 v[0:3], a[104:107], v[64:67], v[0:3]\n\t"
      "ds_read_b128 v[164:167], v216 offset:192\n\t"
      "v_cvt_pk_f16_f32 v104, v4, v5 clamp\n\t"
      "v_mfma_f32_16x16x32_f16 v[0:3], a[108:111], v[68:71], v[0:3]\n\t"
      "v_cvt_pk_f16_f32 v105, v6, v7 clamp\n\t"
      "v_pk_mul_f16 v53, v85, v101\n\t"
      "v_mfma_f32_16x16x32_f16 v[4:7], a[112:115], v[56:59], v[36:39]\n\t"
      "v_pk_fmac_f16 v114, v104, v130\n\t"
      "v_pk_mul_f16 v54, v86, v102\n\t"
      "v_mfma_f32_16x16x32_f16 v[4:7], a[116:119], v[60:63], v[4:7]\n\t"
      "v_pk_fmac_f16 v115, v105, v131\n\t"
      "v_pk_mul_f16 v55, v87, v103\n\t"
      "v_mfma_f32_16x16x32_f16 v[4:7], a[120:123], v[64:67], v[4:7]\n\t"
      "ds_read_b128 v[100:103], v212 offset:4800\n\t"
      "v_cvt_pk_f16_f32 v104, v0, v1 clamp\n\t"
      "v_mfma_f32_16x16x32_f16 v[4:7], a[124:127], v[68:71], v[4:7]\n\t"
      "ds_read_b128 v[168:171], v217 offset:192\n\t"
      "v_cvt_pk_f16_f32 v105, v2, v3 clamp\n\t"
      "v_mfma_f32_16x16x32_f16 v[0:3], a[0:3], v[40:43], v[8:11]\n\t"
      "ds_read_b128 v[180:183], v225 offset:192\n\t"
      "v_pk_fmac_f16 v112, v104, v132\n\t"
      "v_mfma_f32_16x16x32_f16 v[0:3], a[4:7], v[44:47], v[0:3]\n\t"
      "s_waitcnt lgkmcnt(5)\n\t"
      "v_pk_fmac_f16 v113, v105, v133\n\t"
      "v_pk_mul_f16 v56, v72, v88\n\t"
      "v_mfma_f32_16x16x32_f16 v[0:3], a[8:11], v[48:51], v[0:3]\n\t"
      "s_waitcnt lgkmcnt(0)\n\t"
      "v_cvt_pk_f16_f32 v104, v4, v5 clamp\n\t"
      "v_pk_mul_f16 v172, v164, v180\n\t"
      "v_mfma_f32_16x16x32_f16 v[0:3], a[12:15], v[52:55], v[0:3]\n\t"
      "v_cvt_pk_f16_f32 v105, v6, v7 clamp\n\t"
      "v_pk_mul_f16 v57, v73, v89\n\t"
      "v_mfma_f32_16x16x32_f16 v[4:7], a[16:19], v[40:43], v[12:15]\n\t"
      "v_pk_fmac_f16 v114, v104, v134\n\t"
      "v_pk_mul_f16 v173, v165, v181\n\t"
      "v_mfma_f32_16x16x32_f16 v[4:7], a[20:23], v[44:47], v[4:7]\n\t"
      "v_pk_fmac_f16 v115, v105, v135\n\t"
      "v_pk_mul_f16 v58, v74, v90\n\t"
      "v_mfma_f32_16x16x32_f16 v[4:7], a[24:27], v[48:51], v[4:7]\n\t"
      "v_cvt_pk_f16_f32 v104, v0, v1 clamp\n\t"
      "v_pk_mul_f16 v174, v166, v182\n\t"
      "v_mfma_f32_16x16x32_f16 v[4:7], a[28:31], v[52:55], v[4:7]\n\t"
      "v_cvt_pk_f16_f32 v105, v2, v3 clamp\n\t"
      "v_pk_mul_f16 v59, v75, v91\n\t"
      "v_mfma_f32_16x16x32_f16 v[116:119], a[132:135], v[112:115], v[116:119]\n\t"
      "ds_read_b128 v[88:91], v214 offset:0\n\t"
      "v_pk_mul_f16 v175, v167, v183\n\t"
      "v_mfma_f32_16x16x32_f16 v[0:3], a[32:35], v[40:43], v[16:19]\n\t"
      "ds_read_b128 v[72:75], v215 offset:0\n\t"
      "v_pk_mul_f16 v112, v104, v120\n\t"
      "v_mfma_f32_16x16x32_f16 v[0:3], a[36:39], v[44:47], v[0:3]\n\t"
      "v_pk_mul_f16 v113, v105, v121\n\t"
      "v_pk_mul_f16 v60, v76, v92\n\t"
      "v_mfma_f32_16x16x32_f16 v[0:3], a[40:43], v[48:51], v[0:3]\n\t"
      "v_cvt_pk_f16_f32 v104, v4, v5 clamp\n\t"
      "v_pk_mul_f16 v176, v168, v180\n\t"
      "v_mfma_f32_16x16x32_f16 v[0:3], a[44:47], v[52:55], v[0:3]\n\t"
      "v_cvt_pk_f16_f32 v105, v6, v7 clamp\n\t"
      "v_pk_mul_f16 v61, v77, v93\n\t"
      "v_mfma_f32_16x16x32_f16 v[4:7], a[48:51], v[40:43], v[20:23]\n\t"
      "v_pk_mul_f16 v114, v104, v122\n\t"
      "v_pk_mul_f16 v177, v169, v181\n\t"
      "v_mfma_f32_16x16x32_f16 v[4:7], a[52:55], v[44:47], v[4:7]\n\t"
      "v_pk_mul_f16 v115, v105, v123\n\t"
      "v_pk_mul_f16 v62, v78, v94\n\t"
      "v_mfma_f32_16x16x32_f16 v[4:7], a[56:59], v[48:51], v[4:7]\n\t"
      "v_cvt_pk_f16_f32 v104, v0, v1 clamp\n\t"
      "v_pk_mul_f16 v178, v170, v182\n\t"
      "v_mfma_f32_16x16x32_f16 v[4:7], a[60:63], v[52:55], v[4:7]\n\t"
      "v_cvt_pk_f16_f32 v105, v2, v3 clamp\n\t"
      "v_pk_mul_f16 v63, v79, v95\n\t"
      "v_mfma_f32_16x16x32_f16 v[0:3], a[64:67], v[40:43], v[24:27]\n\t"
      "ds_read_b128 v[92:95], v214 offset:64\n\t"
      "v_pk_fmac_f16 v112, v104, v124\n\t"
      "v_mfma_f32_16x16x32_f16 v[0:3], a[68:71], v[44:47], v[0:3]\n\t"
      "ds_read_b128 v[76:79], v215 offset:64\n\t"
      "v_pk_fmac_f16 v113, v105, v125\n\t"
      "v_pk_mul_f16 v64, v80, v96\n\t"
      "v_mfma_f32_16x16x32_f16 v[0:3], a[72:75], v[48:51], v[0:3]\n\t"
      "v_cvt_pk_f16_f32 v104, v4, v5 clamp\n\t"
      "v_pk_mul_f16 v179, v171, v183\n\t"
      "v_mfma_f32_16x16x32_f16 v[184:187], v[172:175], v[164:167], v[184:187]\n\t"
      "v_add_u32 v216, s45, v216\n\t"
      "v_add_u32 v217, s45, v217\n\t"
      "v_mfma_f32_16x16x32_f16 v[0:3], a[76:79], v[52:55], v[0:3]\n\t"
      "v_cvt_pk_f16_f32 v105, v6, v7 clamp\n\t"
      "v_pk_mul_f16 v65, v81, v97\n\t"
      "v_mfma_f32_16x16x32_f16 v[4:7], a[80:83], v[40:43], v[28:31]\n\t"
      "v_pk_fmac_f16 v114, v104, v126\n\t"
      "v_lshl_or_b32 v139, v107, 3, v139\n\t"
      "v_mfma_f32_16x16x32_f16 v[188:191], v[172:175], v[168:171], v[188:191]\n\t"
      "s_waitcnt vmcnt(8)\n\t"
      "ds_write_b128 v220, v[136:139] offset:0\n\t"
      "v_and_b32 v106, s36, v140\n\t"
      "v_mfma_f32_16x16x32_f16 v[4:7], a[84:87], v[44:47], v[4:7]\n\t"
      "v_pk_fmac_f16 v115, v105, v127\n\t"
      "v_pk_mul_f16 v66, v82, v98\n\t"
      "v_mfma_f32_16x16x32_f16 v[4:7], a[88:91], v[48:51], v[4:7]\n\t"
      "v_cvt_pk_f16_f32 v104, v0, v1 clamp\n\t"
      "v_and_b32 v107, s36, v141\n\t"
      "v_mfma_f32_16x16x32_f16 v[192:195], v[176:179], v[168:171], v[192:195]\n\t"
      "v_pk_maximum3_f16 v243, v243, v106, v107\n\t"
      "v_pk_sub_u16 v106, v106, s44 clamp\n\t"
      "v_mfma_f32_16x16x32_f16 v[4:7], a[92:95], v[52:55], v[4:7]\n\t"
      "v_cvt_pk_f16_f32 v105, v2, v3 clamp\n\t"
      "v_pk_mul_f16 v67, v83, v99\n\t"
      "v_mfma_f32_16x16x32_f16 v[0:3], a[96:99], v[40:43], v[32:35]\n\t"
      "ds_read_b128 v[96:99], v214 offset:128\n\t"
      "v_pk_fmac_f16 v112, v104, v128\n\t"
      "v_mfma_f32_16x16x32_f16 v[0:3], a[100:103], v[44:47], v[0:3]\n\t"
      "ds_read_b128 v[80:83], v215 offset:128\n\t"
      "v_pk_fmac_f16 v113, v105, v129\n\t"
      "v_pk_mul_f16 v68, v84, v100\n\t"
      "v_mfma_f32_16x16x32_f16 v[0:3], a[104:107], v[48:51], v[0:3]\n\t"
      "ds_write_b128 v224, v[184:187] offset:0\n\t"
      "v_cvt_pk_f16_f32 v104, v4, v5 clamp\n\t"
      "v_mfma_f32_16x16x32_f16 v[0:3], a[108:111], v[52:55], v[0:3]\n\t"
      "v_cvt_pk_f16_f32 v105, v6, v7 clamp\n\t"
      "v_pk_mul_f16 v69, v85, v101\n\t"
      "v_mfma_f32_16x16x32_f16 v[4:7], a[112:115], v[40:43], v[36:39]\n\t"
      "v_pk_fmac_f16 v114, v104, v130\n\t"
      "v_pk_mul_f16 v70, v86, v102\n\t"
      "v_mfma_f32_16x16x32_f16 v[4:7], a[116:119], v[44:47], v[4:7]\n\t"
      "v_pk_fmac_f16 v115, v105, v131\n\t"
      "v_pk_mul_f16 v71, v87, v103\n\t"
      "v_mfma_f32_16x16x32_f16 v[4:7], a[120:123], v[48:51], v[4:7]\n\t"
      "ds_read_b128 v[100:103], v214 offset:192\n\t"
      "v_cvt_pk_f16_f32 v104, v0, v1 clamp\n\t"
      "v_mfma_f32_16x16x32_f16 v[4:7], a[124:127], v[52:55], v[4:7]\n\t"
      "ds_read_b128 v[84:87], v215 offset:192\n\t"
      "v_cvt_pk_f16_f32 v105, v2, v3 clamp\n\t"
      "v_mfma_f32_16x16x32_f16 v[0:3], a[0:3], v[56:59], v[8:11]\n\t"
      "ds_write_b128 v224, v[188:191] offset:1280\n\t"
      "v_pk_fmac_f16 v112, v104, v132\n\t"
      "v_mfma_f32_16x16x32_f16 v[0:3], a[4:7], v[60:63], v[0:3]\n\t"
      "s_waitcnt lgkmcnt(7)\n\t"
      "v_pk_fmac_f16 v113, v105, v133\n\t"
      "v_pk_mul_f16 v40, v72, v88\n\t"
      "v_mfma_f32_16x16x32_f16 v[0:3], a[8:11], v[64:67], v[0:3]\n\t"
      "v_cvt_pk_f16_f32 v104, v4, v5 clamp\n\t"
      "v_add_u32 v212, s45, v212\n\t"
      "v_mfma_f32_16x16x32_f16 v[0:3], a[12:15], v[68:71], v[0:3]\n\t"
      "v_cvt_pk_f16_f32 v105, v6, v7 clamp\n\t"
      "v_pk_mul_f16 v41, v73, v89\n\t"
      "v_mfma_f32_16x16x32_f16 v[4:7], a[16:19], v[56:59], v[12:15]\n\t"
      "ds_write_b128 v224, v[192:195] offset:2560\n\t"
      "v_pk_fmac_f16 v114, v104, v134\n\t"
      "v_mfma_f32_16x16x32_f16 v[4:7], a[20:23], v[60:63], v[4:7]\n\t"
      "v_pk_fmac_f16 v115, v105, v135\n\t"
      "v_pk_mul_f16 v42, v74, v90\n\t"
      "v_mfma_f32_16x16x32_f16 v[4:7], a[24:27], v[64:67], v[4:7]\n\t"
      "v_cvt_pk_f16_f32 v104, v0, v1 clamp\n\t"
      "v_cndmask_b32 v212, v212, v241, s[50:51]\n\t"
      "v_mfma_f32_16x16x32_f16 v[4:7], a[28:31], v[68:71], v[4:7]\n\t"
      "v_cvt_pk_f16_f32 v105, v2, v3 clamp\n\t"
      "v_pk_mul_f16 v43, v75, v91\n\t"
      "v_mfma_f32_16x16x32_f16 v[116:119], a[136:139], v[112:115], v[116:119]\n\t"
      "ds_read_b128 v[88:91], v214 offset:2304\n\t"
      "v_add_u32 v213, s45, v213\n\t"
      "v_mfma_f32_16x16x32_f16 v[0:3], a[32:35], v[56:59], v[16:19]\n\t"
      "v_pk_mul_f16 v112, v104, v120\n\t"
      "v_cndmask_b32 v213, v213, v240, s[50:51]\n\t"
      "v_mfma_f32_16x16x32_f16 v[0:3], a[36:39], v[60:63], v[0:3]\n\t"
      "v_pk_mul_f16 v113, v105, v121\n\t"
      "v_pk_mul_f16 v44, v76, v92\n\t"
      "v_mfma_f32_16x16x32_f16 v[0:3], a[40:43], v[64:67], v[0:3]\n\t"
      "ds_read_b32 v196, v234 offset:0\n\t"
      "v_cvt_pk_f16_f32 v104, v4, v5 clamp\n\t"
      "v_mfma_f32_16x16x32_f16 v[0:3], a[44:47], v[68:71], v[0:3]\n\t"
      "v_cvt_pk_f16_f32 v105, v6, v7 clamp\n\t"
      "v_pk_mul_f16 v45, v77, v93\n\t"
      "v_mfma_f32_16x16x32_f16 v[4:7], a[48:51], v[56:59], v[20:23]\n\t"
      "ds_read_b32 v197, v235 offset:0\n\t"
      "v_pk_mul_f16 v114, v104, v122\n\t"
      "v_mfma_f32_16x16x32_f16 v[4:7], a[52:55], v[60:63], v[4:7]\n\t"
      "v_pk_mul_f16 v115, v105, v123\n\t"
      "v_pk_mul_f16 v46, v78, v94\n\t"
      "v_mfma_f32_16x16x32_f16 v[4:7], a[56:59], v[64:67], v[4:7]\n\t"
      "ds_read_b32 v198, v236 offset:0\n\t"
      "v_cvt_pk_f16_f32 v104, v0, v1 clamp\n\t"
      "v_mfma_f32_16x16x32_f16 v[4:7], a[60:63], v[68:71], v[4:7]\n\t"
      "v_cvt_pk_f16_f32 v105, v2, v3 clamp\n\t"
      "v_pk_mul_f16 v47, v79, v95\n\t"
      "v_mfma_f32_16x16x32_f16 v[0:3], a[64:67], v[56:59], v[24:27]\n\t"
      "ds_read_b128 v[92:95], v214 offset:2368\n\t"
      "v_pk_fmac_f16 v112, v104, v124\n\t"
      "v_mfma_f32_16x16x32_f16 v[0:3], a[68:71], v[60:63], v[0:3]\n\t"
      "s_waitcnt lgkmcnt(7)\n\t"
      "v_pk_fmac_f16 v113, v105, v125\n\t"
      "v_pk_mul_f16 v48, v80, v96\n\t"
      "v_mfma_f32_16x16x32_f16 v[0:3], a[72:75], v[64:67], v[0:3]\n\t"
      "ds_read_b32 v199, v237 offset:0\n\t"
      "v_cvt_pk_f16_f32 v104, v4, v5 clamp\n\t"
      "v_mfma_f32_16x16x32_f16 v[0:3], a[76:79], v[68:71], v[0:3]\n\t"
      "v_cvt_pk_f16_f32 v105, v6, v7 clamp\n\t"
      "v_pk_mul_f16 v49, v81, v97\n\t"
      "v_mfma_f32_16x16x32_f16 v[4:7], a[80:83], v[56:59], v[28:31]\n\t"
      "ds_read_b32 v200, v238 offset:0\n\t"
      "v_pk_fmac_f16 v114, v104, v126\n\t"
      "v_mfma_f32_16x16x32_f16 v[4:7], a[84:87], v[60:63], v[4:7]\n\t"
      "v_pk_fmac_f16 v115, v105, v127\n\t"
      "v_pk_mul_f16 v50, v82, v98\n\t"
      "v_mfma_f32_16x16x32_f16 v[4:7], a[88:91], v[64:67], v[4:7]\n\t"
      "ds_read_b32 v210, v239 offset:0\n\t"
      "v_cvt_pk_f16_f32 v104, v0, v1 clamp\n\t"
      "v_mfma_f32_16x16x32_f16 v[4:7], a[92:95], v[68:71], v[4:7]\n\t"
      "v_cvt_pk_f16_f32 v105, v2, v3 clamp\n\t"
      "v_pk_mul_f16 v51, v83, v99\n\t"
      "v_mfma_f32_16x16x32_f16 v[0:3], a[96:99], v[56:59], v[32:35]\n\t"
      "ds_read_b128 v[96:99], v214 offset:2432\n\t"
      "v_pk_fmac_f16 v112, v104, v128\n\t"
      "v_mfma_f32_16x16x32_f16 v[0:3], a[100:103], v[60:63], v[0:3]\n\t"
      "v_pk_fmac_f16 v113, v105, v129\n\t"
      "v_pk_mul_f16 v52, v84, v100\n\t"
      "v_mfma_f32_16x16x32_f16 v[0:3], a[104:107], v[64:67], v[0:3]\n\t"
      "v_cvt_pk_f16_f32 v104, v4, v5 clamp\n\t"
      "v_and_b32 v140, s37, v140\n\t"
      "v_mfma_f32_16x16x32_f16 v[0:3], a[108:111], v[68:71], v[0:3]\n\t"
      "v_cvt_pk_f16_f32 v105, v6, v7 clamp\n\t"
      "v_pk_mul_f16 v53, v85, v101\n\t"
      "v_mfma_f32_16x16x32_f16 v[4:7], a[112:115], v[56:59], v[36:39]\n\t"
      "v_pk_fmac_f16 v114, v104, v130\n\t"
      "v_pk_mul_f16 v54, v86, v102\n\t"
      "v_mfma_f32_16x16x32_f16 v[4:7], a[116:119], v[60:63], v[4:7]\n\t"
      "v_pk_fmac_f16 v115, v105, v131\n\t"
      "v_pk_mul_f16 v55, v87, v103\n\t"
      "v_mfma_f32_16x16x32_f16 v[4:7], a[120:123], v[64:67], v[4:7]\n\t"
      "ds_read_b128 v[100:103], v214 offset:2496\n\t"
      "v_cvt_pk_f16_f32 v104, v0, v1 clamp\n\t"
      "v_mfma_f32_16x16x32_f16 v[4:7], a[124:127], v[68:71], v[4:7]\n\t"
      "v_cvt_pk_f16_f32 v105, v2, v3 clamp\n\t"
      "v_pk_sub_u16 v107, v107, s44 clamp\n\t"
      "v_mfma_f32_16x16x32_f16 v[0:3], a[0:3], v[40:43], v[8:11]\n\t"
      "s_waitcnt lgkmcnt(2)\n\t"
      "v_pk_fmac_f16 v112, v104, v132\n\t"
      "v_cndmask_b32 v201, v201, v210, s[52:53]\n\t"
      "v_mfma_f32_16x16x32_f16 v[0:3], a[4:7], v[44:47], v[0:3]\n\t"
      "v_pk_fmac_f16 v113, v105, v133\n\t"
      "v_pk_mul_f16 v56, v72, v88\n\t"
      "v_mfma_f32_16x16x32_f16 v[0:3], a[8:11], v[48:51], v[0:3]\n\t"
      "v_cvt_pk_f16_f32 v104, v4, v5 clamp\n\t"
      "v_and_b32 v141, s37, v141\n\t"
      "v_mfma_f32_16x16x32_f16 v[0:3], a[12:15], v[52:55], v[0:3]\n\t"
      "v_cvt_pk_f16_f32 v105, v6, v7 clamp\n\t"
      "v_pk_mul_f16 v57, v73, v89\n\t"
      "v_mfma_f32_16x16x32_f16 v[4:7], a[16:19], v[40:43], v[12:15]\n\t"
      "v_pk_fmac_f16 v114, v104, v134\n\t"
      "v_lshl_or_b32 v140, v106, 3, v140\n\t"
      "v_mfma_f32_16x16x32_f16 v[4:7], a[20:23], v[44:47], v[4:7]\n\t"
      "v_pk_fmac_f16 v115, v105, v135\n\t"
      "v_pk_mul_f16 v58, v74, v90\n\t"
      "v_mfma_f32_16x16x32_f16 v[4:7], a[24:27], v[48:51], v[4:7]\n\t"
      "v_cvt_pk_f16_f32 v104, v0, v1 clamp\n\t"
      "v_lshl_or_b32 v141, v107, 3, v141\n\t"
      "v_mfma_f32_16x16x32_f16 v[4:7], a[28:31], v[52:55], v[4:7]\n\t"
      "v_cvt_pk_f16_f32 v105, v2, v3 clamp\n\t"
      "v_pk_mul_f16 v59, v75, v91\n\t"
      "v_mfma_f32_16x16x32_f16 v[116:119], a[140:143], v[112:115], v[116:119]\n\t"
      "ds_read_b128 v[88:91], v214 offset:4608\n\t"
      "v_and_b32 v106, s36, v142\n\t"
      "v_mfma_f32_16x16x32_f16 v[0:3], a[32:35], v[40:43], v[16:19]\n\t"
      "v_pk_mul_f16 v112, v104, v120\n\t"
      "v_and_b32 v107, s36, v143\n\t"
      "v_mfma_f32_16x16x32_f16 v[0:3], a[36:39], v[44:47], v[0:3]\n\t"
      "v_pk_mul_f16 v113, v105, v121\n\t"
      "v_pk_mul_f16 v60, v76, v92\n\t"
      "v_mfma_f32_16x16x32_f16 v[0:3], a[40:43], v[48:51], v[0:3]\n\t"
      "v_cvt_pk_f16_f32 v104, v4, v5 clamp\n\t"
      "v_pk_maximum3_f16 v243, v243, v106, v107\n\t"
      "v_mfma_f32_16x16x32_f16 v[0:3], a[44:47], v[52:55], v[0:3]\n\t"
      "v_cvt_pk_f16_f32 v105, v6, v7 clamp\n\t"
      "v_pk_mul_f16 v61, v77, v93\n\t"
      "v_mfma_f32_16x16x32_f16 v[4:7], a[48:51], v[40:43], v[20:23]\n\t"
      "ds_write_b32 v218, v116 offset:768\n\t"
      "v_pk_mul_f16 v114, v104, v122\n\t"
      "v_mfma_f32_16x16x32_f16 v[4:7], a[52:55], v[44:47], v[4:7]\n\t"
      "v_pk_mul_f16 v115, v105, v123\n\t"
      "v_pk_mul_f16 v62, v78, v94\n\t"
      "v_mfma_f32_16x16x32_f16 v[4:7], a[56:59], v[48:51], v[4:7]\n\t"
      "v_cvt_pk_f16_f32 v104, v0, v1 clamp\n\t"
      "v_pk_sub_u16 v106, v106, s44 clamp\n\t"
      "v_mfma_f32_16x16x32_f16 v[4:7], a[60:63], v[52:55], v[4:7]\n\t"
      "v_cvt_pk_f16_f32 v105, v2, v3 clamp\n\t"
      "v_pk_mul_f16 v63, v79, v95\n\t"
      "v_mfma_f32_16x16x32_f16 v[0:3], a[64:67], v[40:43], v[24:27]\n\t"
      "ds_read_b128 v[92:95], v214 offset:4672\n\t"
      "v_pk_fmac_f16 v112, v104, v124\n\t"
      "v_mfma_f32_16x16x32_f16 v[0:3], a[68:71], v[44:47], v[0:3]\n\t"
      "s_waitcnt lgkmcnt(3)\n\t"
      "v_pk_fmac_f16 v113, v105, v125\n\t"
      "v_pk_mul_f16 v64, v80, v96\n\t"
      "v_mfma_f32_16x16x32_f16 v[0:3], a[72:75], v[48:51], v[0:3]\n\t"
      "v_cvt_pk_f16_f32 v104, v4, v5 clamp\n\t"
      "v_and_b32 v142, s37, v142\n\t"
      "v_mfma_f32_16x16x32_f16 v[0:3], a[76:79], v[52:55], v[0:3]\n\t"
      "v_cvt_pk_f16_f32 v105, v6, v7 clamp\n\t"
      "v_pk_mul_f16 v65, v81, v97\n\t"
      "v_mfma_f32_16x16x32_f16 v[4:7], a[80:83], v[40:43], v[28:31]\n\t"
      "v_pk_fmac_f16 v114, v104, v126\n\t"
      "v_pk_sub_u16 v107, v107, s44 clamp\n\t"
      "v_mfma_f32_16x16x32_f16 v[4:7], a[84:87], v[44:47], v[4:7]\n\t"
      "v_pk_fmac_f16 v115, v105, v127\n\t"
      "v_pk_mul_f16 v66, v82, v98\n\t"
      "v_mfma_f32_16x16x32_f16 v[4:7], a[88:91], v[48:51], v[4:7]\n\t"
      "v_cvt_pk_f16_f32 v104, v0, v1 clamp\n\t"
      "v_and_b32 v143, s37, v143\n\t"
      "v_mfma_f32_16x16x32_f16 v[4:7], a[92:95], v[52:55], v[4:7]\n\t"
      "v_cvt_pk_f16_f32 v105, v2, v3 clamp\n\t"
      "v_pk_mul_f16 v67, v83, v99\n\t"
      "v_mfma_f32_16x16x32_f16 v[0:3], a[96:99], v[40:43], v[32:35]\n\t"
      "ds_read_b128 v[96:99], v214 offset:4736\n\t"
      "v_pk_fmac_f16 v112, v104, v128\n\t"
      "v_mfma_f32_16x16x32_f16 v[0:3], a[100:103], v[44:47], v[0:3]\n\t"
      "v_pk_fmac_f16 v113, v105, v129\n\t"
      "v_pk_mul_f16 v68, v84, v100\n\t"
      "v_mfma_f32_16x16x32_f16 v[0:3], a[104:107], v[48:51], v[0:3]\n\t"
      "v_cvt_pk_f16_f32 v104, v4, v5 clamp\n\t"
      "v_lshl_or_b32 v142, v106, 3, v142\n\t"
      "v_mfma_f32_16x16x32_f16 v[0:3], a[108:111], v[52:55], v[0:3]\n\t"
      "v_cvt_pk_f16_f32 v105, v6, v7 clamp\n\t"
      "v_pk_mul_f16 v69, v85, v101\n\t"
      "v_mfma_f32_16x16x32_f16 v[4:7], a[112:115], v[40:43], v[36:39]\n\t"
      "v_pk_fmac_f16 v114, v104, v130\n\t"
      "v_pk_mul_f16 v70, v86, v102\n\t"
      "v_mfma_f32_16x16x32_f16 v[4:7], a[116:119], v[44:47], v[4:7]\n\t"
      "v_pk_fmac_f16 v115, v105, v131\n\t"
      "v_pk_mul_f16 v71, v87, v103\n\t"
      "v_mfma_f32_16x16x32_f16 v[4:7], a[120:123], v[48:51], v[4:7]\n\t"
      "ds_read_b128 v[100:103], v214 offset:4800\n\t"
      "v_cvt_pk_f16_f32 v104, v0, v1 clamp\n\t"
      "v_mfma_f32_16x16x32_f16 v[4:7], a[124:127], v[52:55], v[4:7]\n\t"
      "v_cvt_pk_f16_f32 v105, v2, v3 clamp\n\t"
      "v_lshl_or_b32 v143, v107, 3, v143\n\t"
      "v_mfma_f32_16x16x32_f16 v[0:3], a[0:3], v[56:59], v[8:11]\n\t"
      "v_pk_fmac_f16 v112, v104, v132\n\t"
      "v_add_u32 v215, s45, v215\n\t"
      "v_mfma_f32_16x16x32_f16 v[0:3], a[4:7], v[60:63], v[0:3]\n\t"
      "s_waitcnt lgkmcnt(2)\n\t"
      "v_pk_fmac_f16 v113, v105, v133\n\t"
      "v_pk_mul_f16 v40, v72, v88\n\t"
      "v_mfma_f32_16x16x32_f16 v[0:3], a[8:11], v[64:67], v[0:3]\n\t"
      "v_cvt_pk_f16_f32 v104, v4, v5 clamp\n\t"
      "v_add_u32 v214, s45, v214\n\t"
      "v_mfma_f32_16x16x32_f16 v[0:3], a[12:15], v[68:71], v[0:3]\n\t"
      "v_cvt_pk_f16_f32 v105, v6, v7 clamp\n\t"
      "v_pk_mul_f16 v41, v73, v89\n\t"
      "v_mfma_f32_16x16x32_f16 v[4:7], a[16:19], v[56:59], v[12:15]\n\t"
      "ds_write_b128 v220, v[140:143] offset:1152\n\t"
      "v_pk_fmac_f16 v114, v104, v134\n\t"
      "v_mfma_f32_16x16x32_f16 v[4:7], a[20:23], v[60:63], v[4:7]\n\t"
      "v_pk_fmac_f16 v115, v105, v135\n\t"
      "v_pk_mul_f16 v42, v74, v90\n\t"
      "v_mfma_f32_16x16x32_f16 v[4:7], a[24:27], v[64:67], v[4:7]\n\t"
      "s_waitcnt vmcnt(7)\n\t"
      "v_cvt_pk_f16_f32 v104, v0, v1 clamp\n\t"
      "v_and_b32 v106, s36, v144\n\t"
      "v_mfma_f32_16x16x32_f16 v[4:7], a[28:31], v[68:71], v[4:7]\n\t"
      "v_cvt_pk_f16_f32 v105, v2, v3 clamp\n\t"
      "v_pk_mul_f16 v43, v75, v91\n\t"
      "v_mfma_f32_16x16x32_f16 v[116:119], a[128:131], v[112:115], 0\n\t"
      "ds_read_b128 v[88:91], v233 offset:0\n\t"
      "v_and_b32 v107, s36, v145\n\t"
      "v_mfma_f32_16x16x32_f16 v[0:3], a[32:35], v[56:59], v[16:19]\n\t"
      "ds_read_b128 v[72:75], v232 offset:0\n\t"
      "v_pk_mul_f16 v112, v104, v120\n\t"
      "v_mfma_f32_16x16x32_f16 v[0:3], a[36:39], v[60:63], v[0:3]\n\t"
      "v_pk_mul_f16 v113, v105, v121\n\t"
      "v_pk_mul_f16 v44, v76, v92\n\t"
      "v_mfma_f32_16x16x32_f16 v[0:3], a[40:43], v[64:67], v[0:3]\n\t"
      "v_cvt_pk_f16_f32 v104, v4, v5 clamp\n\t"
      "v_pk_maximum3_f16 v243, v243, v106, v107\n\t"
      "v_mfma_f32_16x16x32_f16 v[0:3], a[44:47], v[68:71], v[0:3]\n\t"
      "v_cvt_pk_f16_f32 v105, v6, v7 clamp\n\t"
      "v_pk_mul_f16 v45, v77, v93\n\t"
      "v_mfma_f32_16x16x32_f16 v[4:7], a[48:51], v[56:59], v[20:23]\n\t"
      "v_pk_mul_f16 v114, v104, v122\n\t"
      "v_pk_sub_u16 v106, v106, s44 clamp\n\t"
      "v_mfma_f32_16x16x32_f16 v[4:7], a[52:55], v[60:63], v[4:7]\n\t"
      "v_pk_mul_f16 v115, v105, v123\n\t"
      "v_pk_mul_f16 v46, v78, v94\n\t"
      "v_mfma_f32_16x16x32_f16 v[4:7], a[56:59], v[64:67], v[4:7]\n\t"
      "v_cvt_pk_f16_f32 v104, v0, v1 clamp\n\t"
      "v_and_b32 v144, s37, v144\n\t"
      "v_mfma_f32_16x16x32_f16 v[4:7], a[60:63], v[68:71], v[4:7]\n\t"
      "v_cvt_pk_f16_f32 v105, v2, v3 clamp\n\t"
      "v_pk_mul_f16 v47, v79, v95\n\t"
      "v_mfma_f32_16x16x32_f16 v[0:3], a[64:67], v[56:59], v[24:27]\n\t"
      "ds_read_b128 v[92:95], v233 offset:64\n\t"
      "v_pk_fmac_f16 v112, v104, v124\n\t"
      "v_mfma_f32_16x16x32_f16 v[0:3], a[68:71], v[60:63], v[0:3]\n\t"
      "s_waitcnt lgkmcnt(4)\n\t"
      "ds_read_b128 v[76:79], v232 offset:64\n\t"
      "v_pk_fmac_f16 v113, v105, v125\n\t"
      "v_pk_mul_f16 v48, v80, v96\n\t"
      "v_mfma_f32_16x16x32_f16 v[0:3], a[72:75], v[64:67], v[0:3]\n\t"
      "v_cvt_pk_f16_f32 v104, v4, v5 clamp\n\t"
      "v_pk_sub_u16 v107, v107, s44 clamp\n\t"
      "v_mfma_f32_16x16x32_f16 v[0:3], a[76:79], v[68:71], v[0:3]\n\t"
      "v_cvt_pk_f16_f32 v105, v6, v7 clamp\n\t"
      "v_pk_mul_f16 v49, v81, v97\n\t"
      "v_mfma_f32_16x16x32_f16 v[4:7], a[80:83], v[56:59], v[28:31]\n\t"
      "v_pk_fmac_f16 v114, v104, v126\n\t"
      "v_and_b32 v145, s37, v145\n\t"
      "v_mfma_f32_16x16x32_f16 v[4:7], a[84:87], v[60:63], v[4:7]\n\t"
      "v_pk_fmac_f16 v115, v105, v127\n\t"
      "v_pk_mul_f16 v50, v82, v98\n\t"
      "v_mfma_f32_16x16x32_f16 v[4:7], a[88:91], v[64:67], v[4:7]\n\t"
      "v_cvt_pk_f16_f32 v104, v0, v1 clamp\n\t"
      "v_lshl_or_b32 v144, v106, 3, v144\n\t"
      "v_mfma_f32_16x16x32_f16 v[4:7], a[92:95], v[68:71], v[4:7]\n\t"
      "v_cvt_pk_f16_f32 v105, v2, v3 clamp\n\t"
      "v_pk_mul_f16 v51, v83, v99\n\t"
      "v_mfma_f32_16x16x32_f16 v[0:3], a[96:99], v[56:59], v[32:35]\n\t"
      "ds_read_b128 v[96:99], v233 offset:128\n\t"
      "v_pk_fmac_f16 v112, v104, v128\n\t"
      "v_mfma_f32_16x16x32_f16 v[0:3], a[100:103], v[60:63], v[0:3]\n\t"
      "ds_read_b128 v[80:83], v232 offset:128\n\t"
      "v_pk_fmac_f16 v113, v105, v129\n\t"
      "v_pk_mul_f16 v52, v84, v100\n\t"
      "v_mfma_f32_16x16x32_f16 v[0:3], a[104:107], v[64:67], v[0:3]\n\t"
      "v_cvt_pk_f16_f32 v104, v4, v5 clamp\n\t"
      "v_lshl_or_b32 v145, v107, 3, v145\n\t"
      "v_mfma_f32_16x16x32_f16 v[0:3], a[108:111], v[68:71], v[0:3]\n\t"
      "v_cvt_pk_f16_f32 v105, v6, v7 clamp\n\t"
      "v_pk_mul_f16 v53, v85, v101\n\t"
      "v_mfma_f32_16x16x32_f16 v[4:7], a[112:115], v[56:59], v[36:39]\n\t"
      "v_pk_fmac_f16 v114, v104, v130\n\t"
      "v_pk_mul_f16 v54, v86, v102\n\t"
      "v_mfma_f32_16x16x32_f16 v[4:7], a[116:119], v[60:63], v[4:7]\n\t"
      "v_pk_fmac_f16 v115, v105, v131\n\t"
      "v_pk_mul_f16 v55, v87, v103\n\t"
      "v_mfma_f32_16x16x32_f16 v[4:7], a[120:123], v[64:67], v[4:7]\n\t"
      "ds_read_b128 v[100:103], v233 offset:192\n\t"
      "v_cvt_pk_f16_f32 v104, v0, v1 clamp\n\t"
      "v_mfma_f32_16x16x32_f16 v[4:7], a[124:127], v[68:71], v[4:7]\n\t"
      "ds_read_b128 v[84:87], v232 offset:192\n\t"
      "v_cvt_pk_f16_f32 v105, v2, v3 clamp\n\t"
      "v_mfma_f32_16x16x32_f16 v[0:3], a[0:3], v[40:43], v[8:11]\n\t"
      "v_pk_fmac_f16 v112, v104, v132\n\t"
      "v_add_u32 v232, s45, v232\n\t"
      "v_mfma_f32_16x16x32_f16 v[0:3], a[4:7], v[44:47], v[0:3]\n\t"
      "s_waitcnt lgkmcnt(4)\n\t"
      "v_pk_fmac_f16 v113, v105, v133\n\t"
      "v_pk_mul_f16 v56, v72, v88\n\t"
      "v_mfma_f32_16x16x32_f16 v[0:3], a[8:11], v[48:51], v[0:3]\n\t"
      "v_cvt_pk_f16_f32 v104, v4, v5 clamp\n\t"
      "v_add_u32 v233, s45, v233\n\t"
      "v_mfma_f32_16x16x32_f16 v[0:3], a[12:15], v[52:55], v[0:3]\n\t"
      "v_cvt_pk_f16_f32 v105, v6, v7 clamp\n\t"
      "v_pk_mul_f16 v57, v73, v89\n\t"
      "v_mfma_f32_16x16x32_f16 v[4:7], a[16:19], v[40:43], v[12:15]\n\t"
      "v_pk_fmac_f16 v114, v104, v134\n\t"
      "v_and_b32 v106, s36, v146\n\t"
      "v_mfma_f32_16x16x32_f16 v[4:7], a[20:23], v[44:47], v[4:7]\n\t"
      "v_pk_fmac_f16 v115, v105, v135\n\t"
      "v_pk_mul_f16 v58, v74, v90\n\t"
      "v_mfma_f32_16x16x32_f16 v[4:7], a[24:27], v[48:51], v[4:7]\n\t"
      "v_cvt_pk_f16_f32 v104, v0, v1 clamp\n\t"
      "v_and_b32 v107, s36, v147\n\t"
      "v_mfma_f32_16x16x32_f16 v[4:7], a[28:31], v[52:55], v[4:7]\n\t"
      "v_cvt_pk_f16_f32 v105, v2, v3 clamp\n\t"
      "v_pk_mul_f16 v59, v75, v91\n\t"
      "v_mfma_f32_16x16x32_f16 v[116:119], a[132:135], v[112:115], v[116:119]\n\t"
      "ds_read_b128 v[88:91], v212 offset:0\n\t"
      "v_pk_maximum3_f16 v243, v243, v106, v107\n\t"
      "v_mfma_f32_16x16x32_f16 v[0:3], a[32:35], v[40:43], v[16:19]\n\t"
      "ds_read_b128 v[72:75], v213 offset:1152\n\t"
      "v_pk_mul_f16 v112, v104, v120\n\t"
      "v_mfma_f32_16x16x32_f16 v[0:3], a[36:39], v[44:47], v[0:3]\n\t"
      "v_pk_mul_f16 v113, v105, v121\n\t"
      "v_pk_mul_f16 v60, v76, v92\n\t"
      "v_mfma_f32_16x16x32_f16 v[0:3], a[40:43], v[48:51], v[0:3]\n\t"
      "v_cvt_pk_f16_f32 v104, v4, v5 clamp\n\t"
      "v_pk_sub_u16 v106, v106, s44 clamp\n\t"
      "v_mfma_f32_16x16x32_f16 v[0:3], a[44:47], v[52:55], v[0:3]\n\t"
      "v_cvt_pk_f16_f32 v105, v6, v7 clamp\n\t"
      "v_pk_mul_f16 v61, v77, v93\n\t"
      "v_mfma_f32_16x16x32_f16 v[4:7], a[48:51], v[40:43], v[20:23]\n\t"
      "v_pk_mul_f16 v114, v104, v122\n\t"
      "v_and_b32 v146, s37, v146\n\t"
      "v_mfma_f32_16x16x32_f16 v[4:7], a[52:55], v[44:47], v[4:7]\n\t"
      "v_pk_mul_f16 v115, v105, v123\n\t"
      "v_pk_mul_f16 v62, v78, v94\n\t"
      "v_mfma_f32_16x16x32_f16 v[4:7], a[56:59], v[48:51], v[4:7]\n\t"
      "v_cvt_pk_f16_f32 v104, v0, v1 clamp\n\t"
      "v_pk_sub_u16 v107, v107, s44 clamp\n\t"
      "v_mfma_f32_16x16x32_f16 v[4:7], a[60:63], v[52:55], v[4:7]\n\t"
      "v_cvt_pk_f16_f32 v105, v2, v3 clamp\n\t"
      "v_pk_mul_f16 v63, v79, v95\n\t"
      "v_mfma_f32_16x16x32_f16 v[0:3], a[64:67], v[40:43], v[24:27]\n\t"
      "ds_read_b128 v[92:95], v212 offset:64\n\t"
      "v_pk_fmac_f16 v112, v104, v124\n\t"
      "v_mfma_f32_16x16x32_f16 v[0:3], a[68:71], v[44:47], v[0:3]\n\t"
      "s_waitcnt lgkmcnt(3)\n\t"
      "ds_read_b128 v[76:79], v213 offset:1216\n\t"
      "v_pk_fmac_f16 v113, v105, v125\n\t"
      "v_pk_mul_f16 v64, v80, v96\n\t"
      "v_mfma_f32_16x16x32_f16 v[0:3], a[72:75], v[48:51], v[0:3]\n\t"
      "v_cvt_pk_f16_f32 v104, v4, v5 clamp\n\t"
      "v_and_b32 v147, s37, v147\n\t"
      "v_mfma_f32_16x16x32_f16 v[0:3], a[76:79], v[52:55], v[0:3]\n\t"
      "v_cvt_pk_f16_f32 v105, v6, v7 clamp\n\t"
      "v_pk_mul_f16 v65, v81, v97\n\t"
      "v_mfma_f32_16x16x32_f16 v[4:7], a[80:83], v[40:43], v[28:31]\n\t"
      "v_pk_fmac_f16 v114, v104, v126\n\t"
      "v_lshl_or_b32 v146, v106, 3, v146\n\t"
      "v_mfma_f32_16x16x32_f16 v[4:7], a[84:87], v[44:47], v[4:7]\n\t"
      "v_pk_fmac_f16 v115, v105, v127\n\t"
      "v_pk_mul_f16 v66, v82, v98\n\t"
      "v_mfma_f32_16x16x32_f16 v[4:7], a[88:91], v[48:51], v[4:7]\n\t"
      "v_cvt_pk_f16_f32 v104, v0, v1 clamp\n\t"
      "v_lshl_or_b32 v147, v107, 3, v147\n\t"
      "v_mfma_f32_16x16x32_f16 v[4:7], a[92:95], v[52:55], v[4:7]\n\t"
      "v_cvt_pk_f16_f32 v105, v2, v3 clamp\n\t"
      "v_pk_mul_f16 v67, v83, v99\n\t"
      "v_mfma_f32_16x16x32_f16 v[0:3], a[96:99], v[40:43], v[32:35]\n\t"
      "ds_read_b128 v[96:99], v212 offset:128\n\t"
      "v_pk_fmac_f16 v112, v104, v128\n\t"
      "v_mfma_f32_16x16x32_f16 v[0:3], a[100:103], v[44:47], v[0:3]\n\t"
      "ds_read_b128 v[80:83], v213 offset:1280\n\t"
      "v_pk_fmac_f16 v113, v105, v129\n\t"
      "v_pk_mul_f16 v68, v84, v100\n\t"
      "v_mfma_f32_16x16x32_f16 v[0:3], a[104:107], v[48:51], v[0:3]\n\t"
      "ds_write_b128 v220, v[144:147] offset:2304\n\t"
      "v_cvt_pk_f16_f32 v104, v4, v5 clamp\n\t"
      "v_mfma_f32_16x16x32_f16 v[0:3], a[108:111], v[52:55], v[0:3]\n\t"
      "v_cvt_pk_f16_f32 v105, v6, v7 clamp\n\t"
      "v_pk_mul_f16 v69, v85, v101\n\t"
      "v_mfma_f32_16x16x32_f16 v[4:7], a[112:115], v[40:43], v[36:39]\n\t"
      "v_pk_fmac_f16 v114, v104, v130\n\t"
      "v_pk_mul_f16 v70, v86, v102\n\t"
      "v_mfma_f32_16x16x32_f16 v[4:7], a[116:119], v[44:47], v[4:7]\n\t"
      "v_pk_fmac_f16 v115, v105, v131\n\t"
      "v_pk_mul_f16 v71, v87, v103\n\t"
      "v_mfma_f32_16x16x32_f16 v[4:7], a[120:123], v[48:51], v[4:7]\n\t"
      "ds_read_b128 v[100:103], v212 offset:192\n\t"
      "v_cvt_pk_f16_f32 v104, v0, v1 clamp\n\t"
      "v_mfma_f32_16x16x32_f16 v[4:7], a[124:127], v[52:55], v[4:7]\n\t"
      "ds_read_b128 v[84:87], v213 offset:1344\n\t"
      "v_cvt_pk_f16_f32 v105, v2, v3 clamp\n\t"
      "v_mfma_f32_16x16x32_f16 v[0:3], a[0:3], v[56:59], v[8:11]\n\t"
      "v_pk_fmac_f16 v112, v104, v132\n\t"
      "v_cndmask_b32 v212, v212, v242, s[50:51]\n\t"
      "v_mfma_f32_16x16x32_f16 v[0:3], a[4:7], v[60:63], v[0:3]\n\t"
      "s_waitcnt lgkmcnt(5)\n\t"
      "v_pk_fmac_f16 v113, v105, v133\n\t"
      "v_pk_mul_f16 v40, v72, v88\n\t"
      "v_mfma_f32_16x16x32_f16 v[0:3], a[8:11], v[64:67], v[0:3]\n\t"
      "v_cvt_pk_f16_f32 v104, v4, v5 clamp\n\t"
      "v_subrev_u32 v213, s47, v213\n\t"
      "v_mfma_f32_16x16x32_f16 v[0:3], a[12:15], v[68:71], v[0:3]\n\t"
      "v_cvt_pk_f16_f32 v105, v6, v7 clamp\n\t"
      "v_pk_mul_f16 v41, v73, v89\n\t"
      "v_mfma_f32_16x16x32_f16 v[4:7], a[16:19], v[56:59], v[12:15]\n\t"
      "s_waitcnt vmcnt(6)\n\t"
      "v_pk_fmac_f16 v114, v104, v134\n\t"
      "v_lshl_add_u32 v136, v248, 8, v228\n\t"
      "v_mfma_f32_16x16x32_f16 v[4:7], a[20:23], v[60:63], v[4:7]\n\t"
      "global_load_dwordx4 v[136:139], v136, s[40:41]\n\t"
      "v_pk_fmac_f16 v115, v105, v135\n\t"
      "v_pk_mul_f16 v42, v74, v90\n\t"
      "v_mfma_f32_16x16x32_f16 v[4:7], a[24:27], v[64:67], v[4:7]\n\t"
      "v_cvt_pk_f16_f32 v104, v0, v1 clamp\n\t"
      "v_lshl_add_u32 v140, v249, 8, v228\n\t"
      "v_mfma_f32_16x16x32_f16 v[4:7], a[28:31], v[68:71], v[4:7]\n\t"
      "v_cvt_pk_f16_f32 v105, v2, v3 clamp\n\t"
      "v_pk_mul_f16 v43, v75, v91\n\t"
      "v_mfma_f32_16x16x32_f16 v[116:119], a[136:139], v[112:115], v[116:119]\n\t"
      "ds_read_b128 v[88:91], v212 offset:0\n\t"
      "global_load_dwordx4 v[140:143], v140, s[40:41]\n\t"
      "v_lshl_add_u32 v144, v250, 8, v228\n\t"
      "v_mfma_f32_16x16x32_f16 v[0:3], a[32:35], v[56:59], v[16:19]\n\t"
      "ds_read_b128 v[72:75], v213 offset:2304\n\t"
      "v_pk_mul_f16 v112, v104, v120\n\t"
      "v_mfma_f32_16x16x32_f16 v[0:3], a[36:39], v[60:63], v[0:3]\n\t"
      "v_pk_mul_f16 v113, v105, v121\n\t"
      "v_pk_mul_f16 v44, v76, v92\n\t"
      "v_mfma_f32_16x16x32_f16 v[0:3], a[40:43], v[64:67], v[0:3]\n\t"
      "global_load_dwordx4 v[144:147], v144, s[40:41]\n\t"
      "v_cvt_pk_f16_f32 v104, v4, v5 clamp\n\t"
      "v_mfma_f32_16x16x32_f16 v[0:3], a[44:47], v[68:71], v[0:3]\n\t"
      "v_cvt_pk_f16_f32 v105, v6, v7 clamp\n\t"
      "v_pk_mul_f16 v45, v77, v93\n\t"
      "v_mfma_f32_16x16x32_f16 v[4:7], a[48:51], v[56:59], v[20:23]\n\t"
      "v_pk_mul_f16 v114, v104, v122\n\t"
      "v_mfma_f32_16x16x32_f16 v[4:7], a[52:55], v[60:63], v[4:7]\n\t"
      "global_load_dwordx3 v[248:250], v226, s[38:39]\n\t"
      "v_pk_mul_f16 v115, v105, v123\n\t"
      "v_pk_mul_f16 v46, v78, v94\n\t"
      "v_mfma_f32_16x16x32_f16 v[4:7], a[56:59], v[64:67], v[4:7]\n\t"
      "s_sub_i32 s45, 0, s45\n\t"
      "s_sub_i32 s46, 0, s46\n\t"
      "v_cvt_pk_f16_f32 v104, v0, v1 clamp\n\t"
      "v_mfma_f32_16x16x32_f16 v[4:7], a[60:63], v[68:71], v[4:7]\n\t"
      "s_add_u32 s48, s48, 1\n\t"
      "v_cvt_pk_f16_f32 v105, v2, v3 clamp\n\t"
      "v_pk_mul_f16 v47, v79, v95\n\t"
      "v_mfma_f32_16x16x32_f16 v[0:3], a[64:67], v[56:59], v[24:27]\n\t"
      "ds_read_b128 v[92:95], v212 offset:64\n\t"
      "s_cmp_eq_u32 s48, 1\n\t"
      "v_pk_fmac_f16 v112, v104, v124\n\t"
      "v_mfma_f32_16x16x32_f16 v[0:3], a[68:71], v[60:63], v[0:3]\n\t"
      "s_waitcnt lgkmcnt(3)\n\t"
      "ds_read_b128 v[76:79], v213 offset:2368\n\t"
      "v_pk_fmac_f16 v113, v105, v125\n\t"
      "v_pk_mul_f16 v48, v80, v96\n\t"
      "v_mfma_f32_16x16x32_f16 v[0:3], a[72:75], v[64:67], v[0:3]\n\t"
      "s_cselect_b32 s58, %[istr], 0\n\t"
      "s_add_u32 s42, s42, s58\n\t"
      "v_cvt_pk_f16_f32 v104, v4, v5 clamp\n\t"
      "v_mfma_f32_16x16x32_f16 v[0:3], a[76:79], v[68:71], v[0:3]\n\t"
      "s_addc_u32 s43, s43, 0\n\t"
      "v_cvt_pk_f16_f32 v105, v6, v7 clamp\n\t"
      "v_pk_mul_f16 v49, v81, v97\n\t"
      "v_mfma_f32_16x16x32_f16 v[4:7], a[80:83], v[56:59], v[28:31]\n\t"
      "s_cmp_eq_u32 s48, 3\n\t"
      "s_cselect_b64 s[50:51], -1, 0\n\t"
      "v_pk_fmac_f16 v114, v104, v126\n\t"
      "v_mfma_f32_16x16x32_f16 v[4:7], a[84:87], v[60:63], v[4:7]\n\t"
      "s_cselect_b32 s47, 1152, 0\n\t"
      "v_pk_fmac_f16 v115, v105, v127\n\t"
      "v_pk_mul_f16 v50, v82, v98\n\t"
      "v_mfma_f32_16x16x32_f16 v[4:7], a[88:91], v[64:67], v[4:7]\n\t"
      "s_lshl_b32 s58, s48, 2\n\t"
      "s_lshl_b32 s52, 0x1000f, s58\n\t"
      "v_cvt_pk_f16_f32 v104, v0, v1 clamp\n\t"
      "v_mfma_f32_16x16x32_f16 v[4:7], a[92:95], v[68:71], v[4:7]\n\t"
      "s_add_u32 m0, s58, 60\n\t"
      "v_cvt_pk_f16_f32 v105, v2, v3 clamp\n\t"
      "v_pk_mul_f16 v51, v83, v99\n\t"
      "v_mfma_f32_16x16x32_f16 v[0:3], a[96:99], v[56:59], v[32:35]\n\t"
      "ds_read_b128 v[96:99], v212 offset:128\n\t"
      "s_and_b32 m0, m0, 63\n\t"
      "v_pk_fmac_f16 v112, v104, v128\n\t"
      "v_mfma_f32_16x16x32_f16 v[0:3], a[100:103], v[60:63], v[0:3]\n\t"
      "ds_read_b128 v[80:83], v213 offset:2432\n\t"
      "v_pk_fmac_f16 v113, v105, v129\n\t"
      "v_pk_mul_f16 v52, v84, v100\n\t"
      "v_mfma_f32_16x16x32_f16 v[0:3], a[104:107], v[64:67], v[0:3]\n\t"
      "v_cvt_pk_f16_f32 v104, v4, v5 clamp\n\t"
      "v_mfma_f32_16x16x32_f16 v[0:3], a[108:111], v[68:71], v[0:3]\n\t"
      "v_cvt_pk_f16_f32 v105, v6, v7 clamp\n\t"
      "v_pk_mul_f16 v53, v85, v101\n\t"
      "v_mfma_f32_16x16x32_f16 v[4:7], a[112:115], v[56:59], v[36:39]\n\t"
      "v_pk_fmac_f16 v114, v104, v130\n\t"
      "v_pk_mul_f16 v54, v86, v102\n\t"
      "v_mfma_f32_16x16x32_f16 v[4:7], a[116:119], v[60:63], v[4:7]\n\t"
      "v_pk_fmac_f16 v115, v105, v131\n\t"
      "v_pk_mul_f16 v55, v87, v103\n\t"
      "v_mfma_f32_16x16x32_f16 v[4:7], a[120:123], v[64:67], v[4:7]\n\t"
      "ds_read_b128 v[100:103], v212 offset:192\n\t"
      "v_cvt_pk_f16_f32 v104, v0, v1 clamp\n\t"
      "v_mfma_f32_16x16x32_f16 v[4:7], a[124:127], v[68:71], v[4:7]\n\t"
      "ds_read_b128 v[84:87], v213 offset:2496\n\t"
      "v_cvt_pk_f16_f32 v105, v2, v3 clamp\n\t"
      "s_cmp_lt_u32 s48, 4\n\t"
      "s_cbranch_scc1 Lloop%=\n\t"
      "v_mfma_f32_16x16x32_f16 v[0:3], a[0:3], v[40:43], v[8:11]\n\t"
      "v_pk_fmac_f16 v112, v104, v132\n\t"
      "v_lshrrev_b32 v106, 7, v247\n\t"
      "v_mfma_f32_16x16x32_f16 v[0:3], a[4:7], v[44:47], v[0:3]\n\t"
      "s_waitcnt lgkmcnt(4)\n\t"
      "v_pk_fmac_f16 v113, v105, v133\n\t"
      "v_pk_mul_f16 v56, v72, v88\n\t"
      "v_mfma_f32_16x16x32_f16 v[0:3], a[8:11], v[48:51], v[0:3]\n\t"
      "v_cvt_pk_f16_f32 v104, v4, v5 clamp\n\t"
      "v_max_u32 v106, 1, v106\n\t"
      "v_mfma_f32_16x16x32_f16 v[0:3], a[12:15], v[52:55], v[0:3]\n\t"
      "v_cvt_pk_f16_f32 v105, v6, v7 clamp\n\t"
      "v_pk_mul_f16 v57, v73, v89\n\t"
      "v_mfma_f32_16x16x32_f16 v[4:7], a[16:19], v[40:43], v[12:15]\n\t"
      "v_pk_fmac_f16 v114, v104, v134\n\t"
      "v_add_u32 v106, s62, v106\n\t"
      "v_mfma_f32_16x16x32_f16 v[4:7], a[20:23], v[44:47], v[4:7]\n\t"
      "v_pk_fmac_f16 v115, v105, v135\n\t"
      "v_pk_mul_f16 v58, v74, v90\n\t"
      "v_mfma_f32_16x16x32_f16 v[4:7], a[24:27], v[48:51], v[4:7]\n\t"
      "v_cvt_pk_f16_f32 v104, v0, v1 clamp\n\t"
      "v_subrev_u32 v106, s63, v106\n\t"
      "v_mfma_f32_16x16x32_f16 v[4:7], a[28:31], v[52:55], v[4:7]\n\t"
      "v_cvt_pk_f16_f32 v105, v2, v3 clamp\n\t"
      "v_pk_mul_f16 v59, v75, v91\n\t"
      "v_mfma_f32_16x16x32_f16 v[116:119], a[140:143], v[112:115], v[116:119]\n\t"
      "v_cmp_gt_u32 s[72:73], 5, v106\n\t"
      "v_cmp_eq_u32 vcc, 0, v247\n\t"
      "v_mfma_f32_16x16x32_f16 v[0:3], a[32:35], v[40:43], v[16:19]\n\t"
      "s_or_b64 s[72:73], s[72:73], vcc\n\t"
      "v_pk_mul_f16 v112, v104, v120\n\t"
      "v_mfma_f32_16x16x32_f16 v[0:3], a[36:39], v[44:47], v[0:3]\n\t"
      "v_pk_mul_f16 v113, v105, v121\n\t"
      "v_pk_mul_f16 v60, v76, v92\n\t"
      "v_mfma_f32_16x16x32_f16 v[0:3], a[40:43], v[48:51], v[0:3]\n\t"
      "s_lshl_b32 s70, s62, 1\n\t"
      "v_cvt_pk_f16_f32 v104, v4, v5 clamp\n\t"
      "v_cmp_gt_u32 vcc, 0x7f80, v247\n\t"
      "v_mfma_f32_16x16x32_f16 v[0:3], a[44:47], v[52:55], v[0:3]\n\t"
      "s_add_i32 s70, s70, s65\n\t"
      "v_cvt_pk_f16_f32 v105, v6, v7 clamp\n\t"
      "v_pk_mul_f16 v61, v77, v93\n\t"
      "v_mfma_f32_16x16x32_f16 v[4:7], a[48:51], v[40:43], v[20:23]\n\t"
      "ds_write_b32 v218, v116 offset:1024\n\t"
      "s_sub_i32 s70, 0, s70\n\t"
      "v_pk_mul_f16 v114, v104, v122\n\t"
      "v_mfma_f32_16x16x32_f16 v[4:7], a[52:55], v[44:47], v[4:7]\n\t"
      "s_mov_b32 s78, vcc_lo\n\t"
      "v_pk_mul_f16 v115, v105, v123\n\t"
      "v_pk_mul_f16 v62, v78, v94\n\t"
      "v_mfma_f32_16x16x32_f16 v[4:7], a[56:59], v[48:51], v[4:7]\n\t"
      "ds_read_b32 v202, v219 offset:0\n\t"
      "s_mov_b32 s79, -1\n\t"
      "v_cvt_pk_f16_f32 v104, v0, v1 clamp\n\t"
      "v_mfma_f32_16x16x32_f16 v[4:7], a[60:63], v[52:55], v[4:7]\n\t"
      "s_sub_i32 s74, s67, 0\n\t"
      "v_cvt_pk_f16_f32 v105, v2, v3 clamp\n\t"
      "v_pk_mul_f16 v63, v79, v95\n\t"
      "v_mfma_f32_16x16x32_f16 v[0:3], a[64:67], v[40:43], v[24:27]\n\t"
      "ds_read_b32 v203, v219 offset:256\n\t"
      "s_cmp_le_u32 s74, 0\n\t"
      "v_pk_fmac_f16 v112, v104, v124\n\t"
      "v_mfma_f32_16x16x32_f16 v[0:3], a[68:71], v[44:47], v[0:3]\n\t"
      "s_waitcnt lgkmcnt(3)\n\t"
      "s_cselect_b32 s79, s79, 0\n\t"
      "v_pk_fmac_f16 v113, v105, v125\n\t"
      "v_pk_mul_f16 v64, v80, v96\n\t"
      "v_mfma_f32_16x16x32_f16 v[0:3], a[72:75], v[48:51], v[0:3]\n\t"
      "ds_read_b32 v204, v219 offset:512\n\t"
      "s_sub_i32 s74, s66, -9\n\t"
      "v_cvt_pk_f16_f32 v104, v4, v5 clamp\n\t"
      "v_mfma_f32_16x16x32_f16 v[0:3], a[76:79], v[52:55], v[0:3]\n\t"
      "s_cmp_le_u32 s74, 13\n\t"
      "v_cvt_pk_f16_f32 v105, v6, v7 clamp\n\t"
      "v_pk_mul_f16 v65, v81, v97\n\t"
      "v_mfma_f32_16x16x32_f16 v[4:7], a[80:83], v[40:43], v[28:31]\n\t"
      "ds_read_b32 v205, v219 offset:768\n\t"
      "s_cselect_b32 s79, s79, 0\n\t"
      "v_pk_fmac_f16 v114, v104, v126\n\t"
      "v_mfma_f32_16x16x32_f16 v[4:7], a[84:87], v[44:47], v[4:7]\n\t"
      "s_sub_i32 s74, s64, -100\n\t"
      "v_pk_fmac_f16 v115, v105, v127\n\t"
      "v_pk_mul_f16 v66, v82, v98\n\t"
      "v_mfma_f32_16x16x32_f16 v[4:7], a[88:91], v[48:51], v[4:7]\n\t"
      "ds_read_b32 v206, v219 offset:1024\n\t"
      "s_cmp_le_u32 s74, 200\n\t"
      "v_cvt_pk_f16_f32 v104, v0, v1 clamp\n\t"
      "v_mfma_f32_16x16x32_f16 v[4:7], a[92:95], v[52:55], v[4:7]\n\t"
      "s_cselect_b32 s79, s79, 0\n\t"
      "v_cvt_pk_f16_f32 v105, v2, v3 clamp\n\t"
      "v_pk_mul_f16 v67, v83, v99\n\t"
      "v_mfma_f32_16x16x32_f16 v[0:3], a[96:99], v[40:43], v[32:35]\n\t"
      "s_sub_i32 s74, s65, -100\n\t"
      "v_pk_fmac_f16 v112, v104, v128\n\t"
      "v_mfma_f32_16x16x32_f16 v[0:3], a[100:103], v[44:47], v[0:3]\n\t"
      "s_cmp_le_u32 s74, 200\n\t"
      "v_pk_fmac_f16 v113, v105, v129\n\t"
      "v_pk_mul_f16 v68, v84, v100\n\t"
      "v_mfma_f32_16x16x32_f16 v[0:3], a[104:107], v[48:51], v[0:3]\n\t"
      "s_cselect_b32 s79, s79, 0\n\t"
      "s_sub_i32 s74, s69, -120\n\t"
      "v_cvt_pk_f16_f32 v104, v4, v5 clamp\n\t"
      "v_mfma_f32_16x16x32_f16 v[0:3], a[108:111], v[52:55], v[0:3]\n\t"
      "s_cmp_le_u32 s74, 240\n\t"
      "v_cvt_pk_f16_f32 v105, v6, v7 clamp\n\t"
      "v_pk_mul_f16 v69, v85, v101\n\t"
      "v_mfma_f32_16x16x32_f16 v[4:7], a[112:115], v[40:43], v[36:39]\n\t"
      "s_cselect_b32 s72, s72, 0\n\t"
      "v_pk_fmac_f16 v114, v104, v130\n\t"
      "v_pk_mul_f16 v70, v86, v102\n\t"
      "v_mfma_f32_16x16x32_f16 v[4:7], a[116:119], v[44:47], v[4:7]\n\t"
      "s_sub_i32 s74, s70, -126\n\t"
      "v_pk_fmac_f16 v115, v105, v131\n\t"
      "v_pk_mul_f16 v71, v87, v103\n\t"
      "v_mfma_f32_16x16x32_f16 v[4:7], a[120:123], v[48:51], v[4:7]\n\t"
      "s_waitcnt lgkmcnt(0)\n\t"
      "s_cmp_le_u32 s74, 252\n\t"
      "v_cvt_pk_f16_f32 v104, v0, v1 clamp\n\t"
      "v_max3_f32 v207, v202, v203, v204\n\t"
      "v_mfma_f32_16x16x32_f16 v[4:7], a[124:127], v[52:55], v[4:7]\n\t"
      "s_cselect_b32 s72, s72, 0\n\t"
      "s_and_b32 s78, s78, %[livem]\n\t"
      "v_cvt_pk_f16_f32 v105, v2, v3 clamp\n\t"
      "v_mfma_f32_16x16x32_f16 v[0:3], a[0:3], v[56:59], v[8:11]\n\t"
      "s_and_b32 s78, s78, s79\n\t"
      "v_pk_fmac_f16 v112, v104, v132\n\t"
      "v_max3_f32 v207, v207, v205, v206\n\t"
      "v_mfma_f32_16x16x32_f16 v[0:3], a[4:7], v[60:63], v[0:3]\n\t"
      "s_and_b32 s72, s72, s78\n\t"
      "v_pk_fmac_f16 v113, v105, v133\n\t"
      "v_max_f32_dpp v207, v207, v207 row_shr:1 row_mask:0xf bank_mask:0xf\n\t"
      "v_mfma_f32_16x16x32_f16 v[0:3], a[8:11], v[64:67], v[0:3]\n\t"
      "s_andn2_b32 s78, s78, s72\n\t"
      "v_cvt_pk_f16_f32 v104, v4, v5 clamp\n\t"
      "v_max_f32_dpp v207, v207, v207 row_shr:2 row_mask:0xf bank_mask:0xf\n\t"
      "v_mfma_f32_16x16x32_f16 v[0:3], a[12:15], v[68:71], v[0:3]\n\t"
      "s_mov_b32 s73, 0\n\t"
      "v_cvt_pk_f16_f32 v105, v6, v7 clamp\n\t"
      "v_max_f32_dpp v207, v207, v207 row_shr:4 row_mask:0xf bank_mask:0xf\n\t"
      "v_mfma_f32_16x16x32_f16 v[4:7], a[16:19], v[56:59], v[12:15]\n\t"
      "s_add_i32 s79, s63, 3\n\t"
      "v_pk_fmac_f16 v114, v104, v134\n\t"
      "v_max_f32_dpp v207, v207, v207 row_shr:8 row_mask:0xf bank_mask:0xf\n\t"
      "v_mfma_f32_16x16x32_f16 v[4:7], a[20:23], v[60:63], v[4:7]\n\t"
      "s_mov_b32 s76, 0x1111ffff\n\t"
      "v_pk_fmac_f16 v115, v105, v135\n\t"
      "v_max_f32_dpp v207, v207, v207 row_bcast:15 row_mask:0xa bank_mask:0xf\n\t"
      "v_mfma_f32_16x16x32_f16 v[4:7], a[24:27], v[64:67], v[4:7]\n\t"
      "s_mov_b32 s77, 0\n\t"
      "v_cvt_pk_f16_f32 v104, v0, v1 clamp\n\t"
      "v_max_f32_dpp v207, v207, v207 row_bcast:31 row_mask:0xc bank_mask:0xf\n\t"
      "v_mfma_f32_16x16x32_f16 v[4:7], a[28:31], v[68:71], v[4:7]\n\t"
      "v_cvt_pk_f16_f32 v105, v2, v3 clamp\n\t"
      "v_readlane_b32 s54, v207, 63\n\t"
      "v_mfma_f32_16x16x32_f16 v[116:119], a[128:131], v[112:115], 0\n\t"
      "v_mbcnt_lo_u32_b32 v107, -1, 0\n\t"
      "v_lshrrev_b32 v107, 1, v107\n\t"
      "v_mfma_f32_16x16x32_f16 v[0:3], a[32:35], v[56:59], v[16:19]\n\t"
      "v_pk_mul_f16 v112, v104, v120\n\t"
      "v_mul_f32 v210, s54, v108\n\t"
      "v_mfma_f32_16x16x32_f16 v[0:3], a[36:39], v[60:63], v[0:3]\n\t"
      "v_pk_mul_f16 v113, v105, v121\n\t"
      "v_writelane_b32 v244, s54, m0\n\t"
      "v_mfma_f32_16x16x32_f16 v[0:3], a[40:43], v[64:67], v[0:3]\n\t"
      "v_cvt_pk_f16_f32 v104, v4, v5 clamp\n\t"
      "v_fma_f32 v202, v202, v108, -v210\n\t"
      "v_mfma_f32_16x16x32_f16 v[0:3], a[44:47], v[68:71], v[0:3]\n\t"
      "v_cvt_pk_f16_f32 v105, v6, v7 clamp\n\t"
      "v_fma_f32 v203, v203, v108, -v210\n\t"
      "v_mfma_f32_16x16x32_f16 v[4:7], a[48:51], v[56:59], v[20:23]\n\t"
      "v_pk_mul_f16 v114, v104, v122\n\t"
      "v_fma_f32 v204, v204, v108, -v210\n\t"
      "v_mfma_f32_16x16x32_f16 v[4:7], a[52:55], v[60:63], v[4:7]\n\t"
      "v_pk_mul_f16 v115, v105, v123\n\t"
      "v_fma_f32 v205, v205, v108, -v210\n\t"
      "v_mfma_f32_16x16x32_f16 v[4:7], a[56:59], v[64:67], v[4:7]\n\t"
      "v_cvt_pk_f16_f32 v104, v0, v1 clamp\n\t"
      "v_fma_f32 v206, v206, v108, -v210\n\t"
      "v_mfma_f32_16x16x32_f16 v[4:7], a[60:63], v[68:71], v[4:7]\n\t"
      "v_cvt_pk_f16_f32 v105, v2, v3 clamp\n\t"
      "v_exp_f32 v202, v202\n\t"
      "v_mfma_f32_16x16x32_f16 v[0:3], a[64:67], v[56:59], v[24:27]\n\t"
      "v_pk_fmac_f16 v112, v104, v124\n\t"
      "v_exp_f32 v203, v203\n\t"
      "v_mfma_f32_16x16x32_f16 v[0:3], a[68:71], v[60:63], v[0:3]\n\t"
      "v_pk_fmac_f16 v113, v105, v125\n\t"
      "v_exp_f32 v204, v204\n\t"
      "v_mfma_f32_16x16x32_f16 v[0:3], a[72:75], v[64:67], v[0:3]\n\t"
      "v_cvt_pk_f16_f32 v104, v4, v5 clamp\n\t"
      "v_exp_f32 v205, v205\n\t"
      "v_mfma_f32_16x16x32_f16 v[0:3], a[76:79], v[68:71], v[0:3]\n\t"
      "v_cvt_pk_f16_f32 v105, v6, v7 clamp\n\t"
      "v_exp_f32 v206, v206\n\t"
      "v_mfma_f32_16x16x32_f16 v[4:7], a[80:83], v[56:59], v[28:31]\n\t"
      "v_pk_fmac_f16 v114, v104, v126\n\t"
      "v_add_f32 v208, v202, v203\n\t"
      "v_mfma_f32_16x16x32_f16 v[4:7], a[84:87], v[60:63], v[4:7]\n\t"
      "v_pk_fmac_f16 v115, v105, v127\n\t"
      "v_mul_f32 v209, v202, v196\n\t"
      "v_mfma_f32_16x16x32_f16 v[4:7], a[88:91], v[64:67], v[4:7]\n\t"
      "v_cvt_pk_f16_f32 v104, v0, v1 clamp\n\t"
      "v_fmac_f32 v209, v203, v197\n\t"
      "v_mfma_f32_16x16x32_f16 v[4:7], a[92:95], v[68:71], v[4:7]\n\t"
      "v_cvt_pk_f16_f32 v105, v2, v3 clamp\n\t"
      "v_add_f32 v208, v208, v204\n\t"
      "v_mfma_f32_16x16x32_f16 v[0:3], a[96:99], v[56:59], v[32:35]\n\t"
      "v_pk_fmac_f16 v112, v104, v128\n\t"
      "v_fmac_f32 v209, v204, v198\n\t"
      "v_mfma_f32_16x16x32_f16 v[0:3], a[100:103], v[60:63], v[0:3]\n\t"
      "v_pk_fmac_f16 v113, v105, v129\n\t"
      "v_add_f32 v208, v208, v205\n\t"
      "v_mfma_f32_16x16x32_f16 v[0:3], a[104:107], v[64:67], v[0:3]\n\t"
      "v_cvt_pk_f16_f32 v104, v4, v5 clamp\n\t"
      "v_fmac_f32 v209, v205, v199\n\t"
      "v_mfma_f32_16x16x32_f16 v[0:3], a[108:111], v[68:71], v[0:3]\n\t"
      "v_cvt_pk_f16_f32 v105, v6, v7 clamp\n\t"
      "v_add_f32 v208, v208, v206\n\t"
      "v_mfma_f32_16x16x32_f16 v[4:7], a[112:115], v[56:59], v[36:39]\n\t"
      "v_pk_fmac_f16 v114, v104, v130\n\t"
      "v_fmac_f32 v209, v206, v200\n\t"
      "v_mfma_f32_16x16x32_f16 v[4:7], a[116:119], v[60:63], v[4:7]\n\t"
      "v_pk_fmac_f16 v115, v105, v131\n\t"
      "v_add_f32_dpp v208, v208, v208 row_shr:1 row_mask:0xf bank_mask:0xf\n\t"
      "v_mfma_f32_16x16x32_f16 v[4:7], a[120:123], v[64:67], v[4:7]\n\t"
      "v_cvt_pk_f16_f32 v104, v0, v1 clamp\n\t"
      "v_add_f32_dpp v209, v209, v209 row_shr:1 row_mask:0xf bank_mask:0xf\n\t"
      "v_mfma_f32_16x16x32_f16 v[4:7], a[124:127], v[68:71], v[4:7]\n\t"
      "v_cvt_pk_f16_f32 v105, v2, v3 clamp\n\t"
      "v_add_f32_dpp v208, v208, v208 row_shr:2 row_mask:0xf bank_mask:0xf\n\t"
      "v_pk_fmac_f16 v112, v104, v132\n\t"
      "v_pk_fmac_f16 v113, v105, v133\n\t"
      "v_add_f32_dpp v209, v209, v209 row_shr:2 row_mask:0xf bank_mask:0xf\n\t"
      "v_add_f32_dpp v208, v208, v208 row_shr:4 row_mask:0xf bank_mask:0xf\n\t"
      "s_nop 1\n\t"
      "v_cvt_pk_f16_f32 v104, v4, v5 clamp\n\t"
      "v_cvt_pk_f16_f32 v105, v6, v7 clamp\n\t"
      "v_pk_fmac_f16 v114, v104, v134\n\t"
      "v_pk_fmac_f16 v115, v105, v135\n\t"
      "v_add_f32_dpp v209, v209, v209 row_shr:4 row_mask:0xf bank_mask:0xf\n\t"
      "v_add_f32_dpp v208, v208, v208 row_shr:8 row_mask:0xf bank_mask:0xf\n\t"
      "v_mfma_f32_16x16x32_f16 v[116:119], a[132:135], v[112:115], v[116:119]\n\t"
      "v_add_f32_dpp v209, v209, v209 row_shr:8 row_mask:0xf bank_mask:0xf\n\t"
      "v_add_f32_dpp v208, v208, v208 row_bcast:15 row_mask:0xa bank_mask:0xf\n\t"
      "s_nop 0\n\t"
      "v_add_f32_dpp v209, v209, v209 row_bcast:15 row_mask:0xa bank_mask:0xf\n\t"
      "v_add_f32_dpp v208, v208, v208 row_bcast:31 row_mask:0xc bank_mask:0xf\n\t"
      "s_nop 0\n\t"
      "v_add_f32_dpp v209, v209, v209 row_bcast:31 row_mask:0xc bank_mask:0xf\n\t"
      "v_readlane_b32 s55, v208, 63\n\t"
      "v_readlane_b32 s56, v209, 63\n\t"
      "s_nop 2\n\t"
      "v_writelane_b32 v245, s55, m0\n\t"
      "v_writelane_b32 v246, s56, m0\n\t"
      "v_mov_b32 v202, 0xff800000\n\t"
      "v_mov_b32_dpp v209, v244 quad_perm:[0,0,0,0] row_mask:0xf bank_mask:0xf\n\t"
      "v_cndmask_b32 v202, v202, v116, s[76:77]\n\t"
      "v_mov_b32 v205, v209\n\t"
      "s_nop 0\n\t"
      "v_max_f32_dpp v203, v202, v202 quad_perm:[1,0,3,2] row_mask:0xf bank_mask:0xf\n\t"
      "v_permlane16_swap_b32 v205, v209\n\t"
      "s_nop 0\n\t"
      "v_max_f32_dpp v203, v203, v203 quad_perm:[2,3,0,1] row_mask:0xf bank_mask:0xf\n\t"
      "v_mov_b32 v204, v203\n\t"
      "s_nop 1\n\t"
      "v_permlane16_swap_b32 v204, v203\n\t"
      "v_max3_f32 v203, v204, v203, v205\n\t"
      "v_sub_f32 v206, v202, v203\n\t"
      "v_sub_f32 v207, v205, v203\n\t"
      "v_mul_f32 v206, v206, v108\n\t"
      "v_mul_f32 v207, v207, v108\n\t"
      "v_exp_f32 v206, v206\n\t"
      "v_exp_f32 v207, v207\n\t"
      "v_mul_f32 v208, v206, v201\n\t"
      "v_add_f32_dpp v206, v206, v206 quad_perm:[1,0,3,2] row_mask:0xf bank_mask:0xf\n\t"
      "s_nop 0\n\t"
      "v_add_f32_dpp v208, v208, v208 quad_perm:[1,0,3,2] row_mask:0xf bank_mask:0xf\n\t"
      "v_add_f32_dpp v206, v206, v206 quad_perm:[2,3,0,1] row_mask:0xf bank_mask:0xf\n\t"
      "s_nop 0\n\t"
      "v_add_f32_dpp v208, v208, v208 quad_perm:[2,3,0,1] row_mask:0xf bank_mask:0xf\n\t"
      "v_mov_b32 v204, v206\n\t"
      "v_mov_b32 v210, v208\n\t"
      "s_nop 0\n\t"
      "v_permlane16_swap_b32 v204, v206\n\t"
      "v_permlane16_swap_b32 v210, v208\n\t"
      "v_add_f32 v206, v204, v206\n\t"
      "v_add_f32 v208, v210, v208\n\t"
      "v_fma_f32 v245, v245, v207, v206\n\t"
      "v_fma_f32 v246, v246, v207, v208\n\t"
      "v_rcp_f32 v245, v245\n\t"
      "s_nop 0\n\t"
      "v_mul_f32 v246, v246, v245\n\t"
      "v_ldexp_f32 v246, v246, s70\n\t"
      "s_mov_b64 exec, s[72:73]\n\t"
      "v_cvt_pk_bf16_f32 v246, v246, v246\n\t"
      "s_cbranch_execz Lnost%=\n\t"
      "global_store_short v107, v246, %[out]\n\t"
      "Lnost%=:\n\t"
      "s_mov_b64 exec, -1\n\t"
      "s_mov_b32 %[okm], s72\n\t"
      "s_mov_b32 %[pend], s78\n\t"
      "s_mov_b32 %[c0], s79\n\t"
      "s_mov_b32 %[pk], s35\n\t"
      "s_waitcnt vmcnt(0) lgkmcnt(0)\n\t"
      "s_branch Lend%=\n\t"
      "Lredo%=:\n\t"
      "s_mov_b64 s[42:43], %[idx0]\n\t"
      "s_mov_b32 s36, 0x7fff7fff\n\t"
      "s_mov_b32 s37, 0x80008000\n\t"
      "s_mov_b64 s[40:41], %[tab]\n\t"
      "s_mov_b32 s49, %[wb]\n\t"
      "s_movk_i32 s61, 288\n\t"
      "s_add_u32 s58, s42, %[istr]\n\t"
      "s_addc_u32 s59, s43, 0\n\t"
      "s_lshl_b32 s34, %[wv], 6\n\t"
      "s_bfe_i32 s64, %[pk], 0x80000\n\t"
      "s_bfe_i32 s65, %[pk], 0x80008\n\t"
      "s_bfe_i32 s79, %[pk], 0x80010\n\t"
      "v_mbcnt_lo_u32_b32 v202, -1, 0\n\t"
      "v_mbcnt_hi_u32_b32 v202, -1, v202\n\t"
      "v_lshrrev_b32 v204, 4, v202\n\t"
      "v_and_b32 v203, 15, v202\n\t"
      "v_mul_u32_u24 v226, 28, v204\n\t"
      "v_min_u32 v226, 80, v226\n\t"
      "v_and_b32 v206, 1, v204\n\t"
      "v_mad_u32_u24 v227, v206, 28, 24\n\t"
      "v_lshlrev_b32 v228, 4, v203\n\t"
      "v_lshl_add_u32 v205, v204, 4, s49\n\t"
      "global_load_dwordx4 v[164:167], v226, s[42:43]\n\t"
      "global_load_dwordx2 v[168:169], v226, s[42:43] offset:16\n\t"
      "global_load_dword v170, v227, s[42:43]\n\t"
      "global_load_dwordx3 v[172:174], v226, s[58:59]\n\t"
      "v_lshlrev_b32 v210, 5, v202\n\t"
      "global_load_dwordx4 v[104:107], v210, %[ltab]\n\t"
      "global_load_dwordx3 v[108:110], v210, %[ltab] offset:16\n\t"
      "v_lshrrev_b32 v8, 2, v203\n\t"
      "v_mad_u32_u24 v212, v8, s61, v205\n\t"
      "v_and_b32 v8, 3, v203\n\t"
      "v_mad_u32_u24 v213, v8, s61, v205\n\t"
      "v_and_b32 v8, 7, v203\n\t"
      "v_mad_u32_u24 v214, v8, s61, v205\n\t"
      "v_lshrrev_b32 v8, 3, v203\n\t"
      "v_add_u32 v8, 24, v8\n\t"
      "v_mad_u32_u24 v215, v8, s61, v205\n\t"
      "v_mad_u32_u24 v216, v203, s61, v205\n\t"
      "v_add_u32 v8, 16, v203\n\t"
      "v_min_u32 v8, 25, v8\n\t"
      "v_mad_u32_u24 v217, v8, s61, v205\n\t"
      "s_add_u32 s54, s49, 24896\n\t"
      "v_lshl_add_u32 v218, v202, 2, s54\n\t"
      "v_mov_b32 v219, v218\n\t"
      "v_add_u32 v9, s49, v228\n\t"
      "v_mad_u32_u24 v220, v204, s61, v9\n\t"
      "v_and_b32 v8, 1, v204\n\t"
      "v_add_u32 v8, 24, v8\n\t"
      "v_mad_u32_u24 v221, v8, s61, v9\n\t"
      "v_add_u32 v222, 14976, v220\n\t"
      "v_add_u32 v223, 9216, v221\n\t"
      "v_mul_u32_u24 v8, 80, v203\n\t"
      "v_add_u32 v8, 26176, v8\n\t"
      "v_add_u32 v224, v8, v205\n\t"
      "v_lshlrev_b32 v8, 4, v204\n\t"
      "v_add_u32 v225, 37760, v8\n\t"
      "v_mad_u32_u24 v229, v203, s61, v8\n\t"
      "v_mul_u32_u24 v207, 2304, v203\n\t"
      "v_lshrrev_b32 v206, 1, v203\n\t"
      "v_add_u32 v206, v206, v204\n\t"
      "v_lshl_add_u32 v209, v206, 4, v207\n\t"
      "v_add_u32 v209, s34, v209\n\t"
      "v_mov_b32 v9, 0x3c003c00\n\t"
      "v_cmp_eq_u32 vcc, 0, v203\n\t"
      "s_nop 1\n\t"
      "v_cndmask_b32 v8, 0, v9, vcc\n\t"
      "v_accvgpr_write_b32 a128, v8\n\t"
      "v_accvgpr_write_b32 a129, v8\n\t"
      "v_accvgpr_write_b32 a130, v8\n\t"
      "v_accvgpr_write_b32 a131, v8\n\t"
      "v_cmp_eq_u32 vcc, 4, v203\n\t"
      "s_nop 1\n\t"
      "v_cndmask_b32 v8, 0, v9, vcc\n\t"
      "v_accvgpr_write_b32 a132, v8\n\t"
      "v_accvgpr_write_b32 a133, v8\n\t"
      "v_accvgpr_write_b32 a134, v8\n\t"
      "v_accvgpr_write_b32 a135, v8\n\t"
      "v_cmp_eq_u32 vcc, 8, v203\n\t"
      "s_nop 1\n\t"
      "v_cndmask_b32 v8, 0, v9, vcc\n\t"
      "v_accvgpr_write_b32 a136, v8\n\t"
      "v_accvgpr_write_b32 a137, v8\n\t"
      "v_accvgpr_write_b32 a138, v8\n\t"
      "v_accvgpr_write_b32 a139, v8\n\t"
      "v_cmp_eq_u32 vcc, 12, v203\n\t"
      "s_nop 1\n\t"
      "v_cndmask_b32 v8, 0, v9, vcc\n\t"
      "v_accvgpr_write_b32 a140, v8\n\t"
      "v_accvgpr_write_b32 a141, v8\n\t"
      "v_accvgpr_write_b32 a142, v8\n\t"
      "v_accvgpr_write_b32 a143, v8\n\t"
      "s_waitcnt vmcnt(0)\n\t"
      "v_lshl_add_u32 v164, v164, 8, v228\n\t"
      "global_load_dwordx4 v[136:139], v164, s[40:41]\n\t"
      "v_lshl_add_u32 v165, v165, 8, v228\n\t"
      "global_load_dwordx4 v[140:143], v165, s[40:41]\n\t"
      "v_lshl_add_u32 v166, v166, 8, v228\n\t"
      "global_load_dwordx4 v[144:147], v166, s[40:41]\n\t"
      "v_lshl_add_u32 v167, v167, 8, v228\n\t"
      "global_load_dwordx4 v[148:151], v167, s[40:41]\n\t"
      "v_lshl_add_u32 v168, v168, 8, v228\n\t"
      "global_load_dwordx4 v[152:155], v168, s[40:41]\n\t"
      "v_lshl_add_u32 v169, v169, 8, v228\n\t"
      "global_load_dwordx4 v[156:159], v169, s[40:41]\n\t"
      "v_lshl_add_u32 v170, v170, 8, v228\n\t"
      "global_load_dwordx4 v[160:163], v170, s[40:41]\n\t"
      "global_load_dwordx3 v[252:254], v226, s[58:59] offset:12\n\t"
      "global_load_dword v251, v227, s[58:59]\n\t"
      "v_lshlrev_b32 v207, 4, v204\n\t"
      "v_lshlrev_b32 v208, 3, v204\n\t"
      "v_mov_b32 v206, 38016\n\t"
      "s_branch Lpost%=\n\t"
      "Lend%=:\n\t"
      "Lafm_codeend:\n\t"
      : [okm] "=s"(okm), [pend] "=s"(pend), [c0] "=s"(c0), "={v247}"(resx), [pk] "+s"(pk)
      : [tab] "s"(table), [idx0] "s"(idxp), [wb] "s"(wb), [W] "s"(W), [b] "s"(b), [h] "s"(h), [p] "s"(p),
        [ii] "s"(ii), [jj] "s"(jj), [ptab] "s"(g_pair_tab), [ltab] "s"(g_lane_tab), [wv] "s"(wave), [out] "s"(outp),
        [livem] "s"(livem), [kf] "s"(kf), [istr] "s"(istr)
      : "memory", "vcc", "scc", "s33", "s34", "s35", "s36", "s37", "s38", "s39", "s40", "s41", "s42", "s43", "s44", "s45", "s46", "s47", "s48", "s49", "s50", "s51", "s52", "s53", "s54", "s55", "s56", "s57", "s58", "s59", "s60", "s61", "s62", "s63", "s64", "s65", "s66", "s67", "s68", "s69", "s70", "s71", "s72", "s73", "s74", "s75", "s76", "s77", "s78", "s79", "v0", "v1", "v2", "v3", "v4", "v5", "v6", "v7", "v8", "v9", "v10", "v11", "v12", "v13", "v14", "v15", "v16", "v17", "v18", "v19", "v20", "v21", "v22", "v23", "v24", "v25", "v26", "v27", "v28", "v29", "v30", "v31", "v32", "v33", "v34", "v35", "v36", "v37", "v38", "v39", "v40", "v41", "v42", "v43", "v44", "v45", "v46", "v47", "v48", "v49", "v50", "v51", "v52", "v53", "v54", "v55", "v56", "v57", "v58", "v59", "v60", "v61", "v62", "v63", "v64", "v65", "v66", "v67", "v68", "v69", "v70", "v71", "v72", "v73", "v74", "v75", "v76", "v77", "v78", "v79", "v80", "v81", "v82", "v83", "v84", "v85", "v86", "v87", "v88", "v89", "v90", "v91", "v92", "v93", "v94", "v95", "v96", "v97", "v98", "v99", "v100", "v101", "v102", "v103", "v104", "v105", "v106", "v107", "v108", "v109", "v110", "v111", "v112", "v113", "v114", "v115", "v116", "v117", "v118", "v119", "v120", "v121", "v122", "v123", "v124", "v125", "v126", "v127", "v128", "v129", "v130", "v131", "v132", "v133", "v134", "v135", "v136", "v137", "v138", "v139", "v140", "v141", "v142", "v143", "v144", "v145", "v146", "v147", "v148", "v149", "v150", "v151", "v152", "v153", "v154", "v155", "v156", "v157", "v158", "v159", "v160", "v161", "v162", "v163", "v164", "v165", "v166", "v167", "v168", "v169", "v170", "v171", "v172", "v173", "v174", "v175", "v176", "v177", "v178", "v179", "v180", "v181", "v182", "v183", "v184", "v185", "v186", "v187", "v188", "v189", "v190", "v191", "v192", "v193", "v194", "v195", "v196", "v197", "v198", "v199", "v200", "v201", "v202", "v203", "v204", "v205", "v206", "v207", "v208", "v209", "v210", "v211", "v212", "v213", "v214", "v215", "v216", "v217", "v218", "v219", "v220", "v221", "v222", "v223", "v224", "v225", "v226", "v227", "v228", "v229", "v230", "v231", "v232", "v233", "v234", "v235", "v236", "v237", "v238", "v239", "v240", "v241", "v242", "v243", "v244", "v245", "v246", "v248", "v249", "v250", "v251", "v252", "v253", "v254", "a0", "a1", "a2", "a3", "a4", "a5", "a6", "a7", "a8", "a9", "a10", "a11", "a12", "a13", "a14", "a15", "a16", "a17", "a18", "a19", "a20", "a21", "a22", "a23", "a24", "a25", "a26", "a27", "a28", "a29", "a30", "a31", "a32", "a33", "a34", "a35", "a36", "a37", "a38", "a39", "a40", "a41", "a42", "a43", "a44", "a45", "a46", "a47", "a48", "a49", "a50", "a51", "a52", "a53", "a54", "a55", "a56", "a57", "a58", "a59", "a60", "a61", "a62", "a63", "a64", "a65", "a66", "a67", "a68", "a69", "a70", "a71", "a72", "a73", "a74", "a75", "a76", "a77", "a78", "a79", "a80", "a81", "a82", "a83", "a84", "a85", "a86", "a87", "a88", "a89", "a90", "a91", "a92", "a93", "a94", "a95", "a96", "a97", "a98", "a99", "a100", "a101", "a102", "a103", "a104", "a105", "a106", "a107", "a108", "a109", "a110", "a111", "a112", "a113", "a114", "a115", "a116", "a117", "a118", "a119", "a120", "a121", "a122", "a123", "a124", "a125", "a126", "a127", "a128", "a129", "a130", "a131", "a132", "a133", "a134", "a135", "a136", "a137", "a138", "a139", "a140", "a141", "a142", "a143");
    if (cur < 0) {
      done = okm;
      if (pend == 0) break;
#pragma unroll
      for (int s = 0; s < 4; ++s) {
        const uint32_t e = (uint32_t)__builtin_amdgcn_readlane((int)resx, 4 * s) >> 7;
        keep |= (e > 1u ? e : 1u) << (8 * s);
      }
      done |= pend << 16;
    } else {
      done |= (okm & 1u) << (4 * cur);
    }
    const uint32_t todo = done >> 16;
    if (todo == 0) break;
    cur = __builtin_amdgcn_readfirstlane(__builtin_ctz(todo) >> 2);
    done = __builtin_amdgcn_readfirstlane(done & ~(0x10000u << (4 * cur)));
    keep = __builtin_amdgcn_readfirstlane(keep);
    kf = __builtin_amdgcn_readfirstlane(c0 - (int)((keep >> (8 * cur)) & 0xffu));
    idxp = idx + (size_t)(sid0 + cur) * F;
    outp = out + sid0 + cur;
    livem = 1u;
    istr = 0;
  }
  done &= 0xffffu;
  const int ln = __builtin_amdgcn_mbcnt_hi(~0u, __builtin_amdgcn_mbcnt_lo(~0u, 0));
  if (done != own) {
    for (int s = 0; s < 4; ++s)
      if (((own & ~done) >> (4 * s)) & 1) slow_sample(idx, table, ii, jj, W, b, h, p, (float*)(lds + wb + RING), sid0 + s, ln, out);
  }
}

__global__ __launch_bounds__(64) void afm_small_kernel(const int* __restrict__ idx, const uint16_t* __restrict__ table,
    const int* __restrict__ ii, const int* __restrict__ jj, const uint16_t* __restrict__ W, const uint16_t* __restrict__ b,
    const uint16_t* __restrict__ h, const uint16_t* __restrict__ p, uint16_t* __restrict__ out) {
  extern __shared__ __attribute__((aligned(16))) char lds[];
  slow_sample(idx, table, ii, jj, W, b, h, p, (float*)lds, blockIdx.x, threadIdx.x, out);
}
}

extern "C" void kernel_launch(void* const* d_in, const int* in_sizes, int n_in, void* d_out, int out_size, void* d_ws, size_t ws_size,
                              hipStream_t stream) {
  const int* idx = (const int*)d_in[0];
  const uint16_t* table = (const uint16_t*)d_in[1];
  const int* ii = (const int*)d_in[2];
  const int* jj = (const int*)d_in[3];
  const uint16_t* W = (const uint16_t*)d_in[4];
  const uint16_t* b = (const uint16_t*)d_in[5];
  const uint16_t* h = (const uint16_t*)d_in[6];
  const uint16_t* p = (const uint16_t*)d_in[7];
  const int B = out_size;
  if (B <= 0) return;
  if (B < 4) {
    afm_small_kernel<<<B, 64, F * D * sizeof(float), stream>>>(idx, table, ii, jj, W, b, h, p, (uint16_t*)d_out);
    return;
  }
  (void)hipFuncSetAttribute((const void*)afm_kernel, hipFuncAttributeMaxDynamicSharedMemorySize, LDS_TOTAL);
  const int ngrp = (B + 3) / 4;
  afm_kernel<<<(ngrp + 3) / 4, 256, LDS_TOTAL, stream>>>(idx, table, ii, jj, W, b, h, p, (uint16_t*)d_out, B);
}
```
